```python
import torch

def _softmax(x, axis=-1):
    return torch.softmax(x, dim=axis)
def _gelu(x):
    return torch.nn.functional.gelu(x, approximate="tanh")
def _silu(x):
    return torch.nn.functional.silu(x)
def _sigmoid(x):
    return torch.sigmoid(x)
def _layernorm(x, axis=-1, eps=1e-5):
    m = x.mean(dim=axis, keepdim=True)
    v = x.var(dim=axis, keepdim=True, unbiased=False)
    return (x - m) / torch.sqrt(v + eps)
def _scatter_add(tgt, idx, src, axis=0):
    return tgt.clone().index_add_(axis, idx, src)
def _segment_sum(src, seg, n):
    out = torch.zeros((n,) + src.shape[1:], dtype=src.dtype)
    return out.index_add_(0, seg, src)
def _pad(x, pw):
    flat = tuple(v for pair in reversed(pw) for v in pair)
    return torch.nn.functional.pad(x, flat)

kernel_name = 'gated_mix__linear_attn_block__grouped_kv_mqa__prompt'

def setup_inputs():

    _w = (lambda a: (lambda t: t.to(torch.bfloat16) if t.is_floating_point() else t)(torch.as_tensor(a)))
    def _A_setup():
        B, S, D = 1, 2048, 512
        return {
            "x": _w((lambda *s: torch.randn(*s, dtype=torch.bfloat16) * 0.05)(B, S, D)),
            "wqkv": _w((lambda *s: torch.randn(*s, dtype=torch.bfloat16) * 1.6)(D, 3 * D)),
            "wo": _w((lambda *s: torch.randn(*s, dtype=torch.bfloat16) * 0.05)(D, D)),
        }
    _A_in = _A_setup()

    def _B_setup():
        B, S, D, H = 1, 2048, 512, 32
        Dh = D // H
        return {
            "x": _w((lambda *s: torch.randn(*s, dtype=torch.bfloat16) * 0.05)(B, S, D)),
            "wq": _w((lambda *s: torch.randn(*s, dtype=torch.bfloat16) * 1.6)(D, H * Dh)),
            "wk": _w((lambda *s: torch.randn(*s, dtype=torch.bfloat16) * 1.6)(D, Dh)),
            "wv": _w((lambda *s: torch.randn(*s, dtype=torch.bfloat16) * 0.05)(D, Dh)),
            "wo": _w((lambda *s: torch.randn(*s, dtype=torch.bfloat16) * 0.05)(H * Dh, D)),
        }
    _B_in = _B_setup()

    return {
        "A_x": _A_in["x"],
        "A_wqkv": _A_in["wqkv"],
        "A_wo": _A_in["wo"],
        "B_x": _B_in["x"],
        "B_wq": _B_in["wq"],
        "B_wk": _B_in["wk"],
        "B_wv": _B_in["wv"],
        "B_wo": _B_in["wo"],
        "gate_w": _w((lambda *s: torch.randn(*s, dtype=torch.bfloat16) * 0.05)(512) * 16.0),
    }

def reference(A_x, A_wqkv, A_wo, B_x, B_wq, B_wk, B_wv, B_wo, gate_w):
    def _A_ref(x, wqkv, wo):
        B, S, D = x.shape
        H = 32; Dh = D // H
        qkv = (lambda x, *d: x.permute(*d))((x @ wqkv).reshape(B, S, 3, H, Dh), 2, 0, 3, 1, 4)
        q, k, v = qkv[0], qkv[1], qkv[2]
        phi = lambda t: torch.where(t > 0, t + 1, torch.exp(t)) + 1e-6
        pq, pk = phi(q), phi(k)
        kv = torch.einsum("bhsd,bhse->bhde", pk, v)
        z = torch.einsum("bhsd->bhd", pk)
        num = torch.einsum("bhqd,bhde->bhqe", pq, kv)
        den = torch.einsum("bhqd,bhd->bhq", pq, z)[..., None] + 1e-6
        o = num / den
        return (lambda x, *d: x.permute(*d))(o, 0, 2, 1, 3).reshape(B, S, D) @ wo

    def _B_ref(x, wq, wk, wv, wo):
        B, S, D = x.shape
        H = 32
        Dh = D // H
        q = (lambda x, *d: x.permute(*d))((x @ wq).reshape(B, S, H, Dh), 0, 2, 1, 3)
        k = x @ wk
        v = x @ wv
        scores = torch.einsum("bhqd,bkd->bhqk", q, k) * (Dh ** -0.5)
        p = _softmax(scores, axis=-1)
        o = torch.einsum("bhqk,bkd->bhqd", p, v)
        return (lambda x, *d: x.permute(*d))(o, 0, 2, 1, 3).reshape(B, S, H * Dh) @ wo

    x = A_x
    g = _sigmoid((x * gate_w).sum(-1, keepdims=True))
    a = _A_ref(x, A_wqkv, A_wo)
    b = _B_ref(x, B_wq, B_wk, B_wv, B_wo)
    return g * a + (1 - g) * b
```

```cpp
#include <hip/hip_runtime.h>
#include <hip/hip_bf16.h>
#include <stdint.h>
#include <type_traits>

typedef __attribute__((ext_vector_type(8))) __bf16 bf16x8;
typedef __attribute__((ext_vector_type(16))) __bf16 bf16x16;
typedef __attribute__((ext_vector_type(2))) __bf16 bf16x2;
typedef __attribute__((ext_vector_type(8))) float f32x8;
typedef __attribute__((ext_vector_type(16))) float f32x16;
typedef __attribute__((ext_vector_type(4))) float f32x4;
typedef __attribute__((ext_vector_type(2))) float f32x2;
typedef __attribute__((ext_vector_type(2))) unsigned u32x2;
typedef __attribute__((ext_vector_type(4))) unsigned u32x4;
typedef __attribute__((ext_vector_type(8))) unsigned u32x8;
typedef __attribute__((ext_vector_type(16))) unsigned u32x16;
typedef __attribute__((ext_vector_type(4))) short s16x4;
typedef unsigned long long u64;
typedef __amdgpu_buffer_rsrc_t rsrc_t;
#define LAS __attribute__((address_space(3)))
#define DEVI __device__ __forceinline__
namespace {
constexpr unsigned WS_EPOCH = 0;
constexpr unsigned WS_FLAG = 4096;
constexpr unsigned WS_KV = WS_FLAG + 5 * 256 * 128;
constexpr unsigned WS_KVP = WS_KV + 131072;
constexpr unsigned WS_KVS = WS_KVP + 256 * 4352;
constexpr unsigned WS_G = WS_KVS + 8 * 4352 + 2048;
constexpr unsigned WS_OA = WS_G + 16384;
constexpr unsigned WS_OB = WS_OA + 2097152;
constexpr unsigned WS_END = WS_OB + 2097152;
#define LOOK_C 9
#define TOUCH 2
#define TOUCH_MIN 0
#define HGOFF 0
#define OB_POS 0
#define RETRY_SLEEP 2
enum { F_KV = 0, F_P = 1, F_S = 2, F_OA = 3, F_OB = 4 };
constexpr unsigned L_WKV = 0, L_X0 = 32768, L_RING = 65536, L_X1 = 131072, L_E = 131072;
constexpr unsigned L_GT = L_E + 18432 + 64, L_PQ = L_GT + 256;
constexpr unsigned RP = 136;
constexpr unsigned L_SCR = 98304;
constexpr unsigned L_GATE = L_WKV + 16384;
DEVI constexpr unsigned slot_off(int c) { return (c & 3) == 0 ? 32768u : (c & 3) == 1 ? 49152u : (c & 3) == 2 ? 0u : 16384u; }

DEVI unsigned pk2(float a, float b) { f32x2 v = {a, b}; return __builtin_bit_cast(unsigned, __builtin_convertvector(v, bf16x2)); }
DEVI f32x4 mf16(u32x4 a, u32x4 b, f32x4 c) {
  return __builtin_amdgcn_mfma_f32_16x16x32_bf16(__builtin_bit_cast(bf16x8, a), __builtin_bit_cast(bf16x8, b), c, 0, 0, 0);
}
DEVI f32x16 mf32(u32x4 a, u32x4 b, f32x16 c) {
  return __builtin_amdgcn_mfma_f32_32x32x16_bf16(__builtin_bit_cast(bf16x8, a), __builtin_bit_cast(bf16x8, b), c, 0, 0, 0);
}
DEVI f32x16 smf32(u32x4 a, f32x8 p, f32x16 c) {
  return __builtin_amdgcn_smfmac_f32_32x32x32_bf16(__builtin_bit_cast(bf16x8, a), __builtin_bit_cast(bf16x16, p), c, 0xdddd, 0, 0);
}
DEVI u32x4 lds128(unsigned a) { return *(const LAS u32x4*)a; }
DEVI u32x2 ldstr(unsigned a) { return __builtin_bit_cast(u32x2, __builtin_amdgcn_ds_read_tr16_b64_v4i16((LAS s16x4*)a)); }
DEVI u32x4 ldstr2(unsigned a, unsigned b) { u32x2 x = ldstr(a), y = ldstr(b); return (u32x4){x[0], x[1], y[0], y[1]}; }
DEVI rsrc_t mkr(const void* p, unsigned bytes) { return __builtin_amdgcn_make_buffer_rsrc((void*)p, 0, bytes, 0x00020000); }
DEVI void dma(rsrc_t r, unsigned voff, unsigned soff, unsigned m0a) {
  asm volatile("s_mov_b32 m0, %3\n\ts_nop 0\n\tbuffer_load_dwordx4 %0, %1, %2 offen lds" :: "v"(voff), "s"(r), "s"(soff), "s"(m0a) : "memory");
}
#define DMA_STEP "\n\ts_add_u32 m0, m0, 0x400\n\ts_add_u32 %0, %0, %5\n\tbuffer_load_dwordx4 %1, %2, %0 offen lds"
DEVI void dma4s(rsrc_t r, unsigned voff, unsigned soff, unsigned m0a, unsigned step) {
  unsigned t;
  asm volatile("s_mov_b32 m0, %4\n\ts_mov_b32 %0, %3\n\tbuffer_load_dwordx4 %1, %2, %0 offen lds" DMA_STEP DMA_STEP DMA_STEP
               : "=&s"(t) : "v"(voff), "s"(r), "s"(soff), "s"(m0a), "s"(step) : "memory", "scc");
}
DEVI void dma_dev(rsrc_t r, unsigned voff, unsigned soff, unsigned m0a) {
  asm volatile("s_mov_b32 m0, %3\n\ts_nop 0\n\tbuffer_load_dwordx4 %0, %1, %2 offen sc1 lds" :: "v"(voff), "s"(r), "s"(soff), "s"(m0a) : "memory");
}
DEVI void dma8s(rsrc_t r, unsigned voff, unsigned soff, unsigned m0a, unsigned step) {
  unsigned t;
  asm volatile("s_mov_b32 m0, %4\n\ts_mov_b32 %0, %3\n\tbuffer_load_dwordx4 %1, %2, %0 offen lds" DMA_STEP DMA_STEP DMA_STEP DMA_STEP DMA_STEP DMA_STEP DMA_STEP
               : "=&s"(t) : "v"(voff), "s"(r), "s"(soff), "s"(m0a), "s"(step) : "memory", "scc");
}
#define DMA_LIN4(SC) "buffer_load_dwordx4 %1, %2, %0 offen " SC "lds\n\tbuffer_load_dwordx4 %1, %2, %0 offen offset:1024 " SC "lds\n\t" \
                     "buffer_load_dwordx4 %1, %2, %0 offen offset:2048 " SC "lds\n\tbuffer_load_dwordx4 %1, %2, %0 offen offset:3072 " SC "lds"
DEVI void dma4(rsrc_t r, unsigned voff, unsigned soff, unsigned m0a) {
  asm volatile("s_mov_b32 m0, %3\n\ts_nop 0\n\t" DMA_LIN4("") :: "s"(soff), "v"(voff), "s"(r), "s"(m0a) : "memory");
}
DEVI void dma8(rsrc_t r, unsigned voff, unsigned soff, unsigned m0a) {
  unsigned t;
  asm volatile("s_mov_b32 m0, %4\n\ts_mov_b32 %0, %3\n\t" DMA_LIN4("") "\n\ts_add_u32 m0, m0, 0x1000\n\ts_add_u32 %0, %0, 0x1000\n\t" DMA_LIN4("")
               : "=&s"(t) : "v"(voff), "s"(r), "s"(soff), "s"(m0a) : "memory", "scc");
}
DEVI void dma8_dev(rsrc_t r, unsigned voff, unsigned soff, unsigned m0a) {
  unsigned t;
  asm volatile("s_mov_b32 m0, %4\n\ts_mov_b32 %0, %3\n\t" DMA_LIN4("sc1 ") "\n\ts_add_u32 m0, m0, 0x1000\n\ts_add_u32 %0, %0, 0x1000\n\t" DMA_LIN4("sc1 ")
               : "=&s"(t) : "v"(voff), "s"(r), "s"(soff), "s"(m0a) : "memory", "scc");
}
DEVI float xhalf_max(float v) {
  auto r = __builtin_amdgcn_permlane32_swap(__float_as_uint(v), __float_as_uint(v), false, false);
  return fmaxf(__uint_as_float(r[0]), __uint_as_float(r[1]));
}
#define WAIT_VM(n) asm volatile("s_waitcnt vmcnt(" #n ")" ::: "memory")
constexpr unsigned OOB = 0x7ffffff0u;
#define BAR() asm volatile("s_waitcnt lgkmcnt(0)\n\ts_barrier" ::: "memory")
DEVI unsigned flag_off(int stage, int tb, int hg) { return WS_FLAG + (unsigned)(stage * 256 + tb * 8 + hg) * 128u; }
DEVI unsigned kv_flag(int tb) { return WS_FLAG + tb * 128u; }
DEVI void put_flag(rsrc_t wr, unsigned off, u32x2 tag) { __builtin_amdgcn_raw_buffer_store_b64(tag, wr, off, 0, 16); }
DEVI void poll(rsrc_t wr, unsigned voff, u32x2 tag) {
#pragma unroll 1
  for (unsigned spins = 0; spins < (1u << 20); ++spins) {
    u32x2 f = __builtin_amdgcn_raw_buffer_load_b64(wr, voff, 0, 16);
    if (__builtin_amdgcn_ballot_w64(__builtin_bit_cast(u64, f) != __builtin_bit_cast(u64, tag)) == 0) break;
    __builtin_amdgcn_s_sleep(1);
  }
  asm volatile("" ::: "memory");
}
DEVI void poll2(rsrc_t wr, unsigned voff, u32x2 tag) {
#pragma unroll 1
  for (unsigned spins = 0; spins < (1u << 20); ++spins) {
    u32x4 f = __builtin_amdgcn_raw_buffer_load_b128(wr, voff, 0, 16);
    if (__builtin_amdgcn_ballot_w64(((f[0] ^ tag[0]) | (f[1] ^ tag[1]) | (f[2] ^ tag[0]) | (f[3] ^ tag[1])) != 0) == 0) break;
    __builtin_amdgcn_s_sleep(1);
  }
  asm volatile("" ::: "memory");
}
DEVI float phi(float t) {
  float e;
  asm("v_exp_f32_e64 %0, %1 clamp\n\ts_nop 0" : "=v"(e) : "v"(t * 1.4426950408889634f));
  return fmaxf(e, t + 1.f) + 1e-6f;
}
DEVI u32x4 pk8(const f32x16& v, int o, float s) {
  return (u32x4){pk2(v[o] * s, v[o + 1] * s), pk2(v[o + 2] * s, v[o + 3] * s), pk2(v[o + 4] * s, v[o + 5] * s), pk2(v[o + 6] * s, v[o + 7] * s)};
}
DEVI u32x4 pk8phi(const f32x16& v, int o) {
  return (u32x4){pk2(phi(v[o]), phi(v[o + 1])), pk2(phi(v[o + 2]), phi(v[o + 3])), pk2(phi(v[o + 4]), phi(v[o + 5])), pk2(phi(v[o + 6]), phi(v[o + 7]))};
}

__global__ void __launch_bounds__(512, 2) fused_kernel(const char* __restrict__ x, const char* __restrict__ wqkv, const char* __restrict__ woa,
                                                       const char* __restrict__ wq, const char* __restrict__ wk, const char* __restrict__ wv,
                                                       const char* __restrict__ wob, const char* __restrict__ gate, char* __restrict__ ws,
                                                       char* __restrict__ out) {
  __shared__ __attribute__((aligned(1024))) char sm[163840];
  const int tid = threadIdx.x, lane = tid & 63, w = __builtin_amdgcn_readfirstlane(tid >> 6);
  const int tb = (blockIdx.x & 7) * 4 + (blockIdx.x >> 6), hg = ((blockIdx.x >> 3) + HGOFF) & 7;
  const unsigned smb = (unsigned)(size_t)(LAS char*)sm;
  const int l31 = lane & 31, h = lane >> 5, l15 = lane & 15, g = lane >> 4;
  const bool loader = w >= 4;
  const rsrc_t wsr = mkr(ws, WS_END);
  const f32x16 zero16 = {0, 0, 0, 0, 0, 0, 0, 0, 0, 0, 0, 0, 0, 0, 0, 0};
  asm volatile("" :: "s"(woa), "s"(wob), "s"(out), "s"(gate), "s"(wk), "s"(wv));

#define ROT 3
  const int rot = ROT == 0 ? (((tb & 3) << 2) | ((tb >> 2) & 3)) : ROT == 1 ? 2 * (tb >> 2) : ROT == 2 ? ((2 * (tb >> 2) + 8 * (tb & 1)) & 15) : ROT == 3 ? (2 * (tb >> 2) + (tb & 1)) : ROT == 4 ? ((2 * (tb >> 2) + (tb & 3)) & 15) : ROT == 5 ? (4 * ((tb >> 2) & 3) + (tb & 3)) : ((2 * (tb >> 2) + (tb & 1) + 2 * ((tb >> 1) & 1) * 0 + ((tb >> 1) & 1) * 8) & 15);
  const int p = w & 3;
  const unsigned wstride = p == 0 ? 1024u : 3072u;
  const rsrc_t wr = mkr((p == 0 ? wq : wqkv + (p - 1) * 1024) + hg * 128, 511u * wstride + 128u);
  const unsigned wvo0 = (unsigned)(4 * h + ((lane >> 2) & 3)) * wstride + (g & 1) * 64 + (lane & 3) * 16, wvo1 = wvo0 ^ 32;
  const unsigned half = (w >> 2) * 2;
  auto issue_share = [&](int c) {
    const unsigned so = ((unsigned)((c + rot) & 15) * 32u + half * 8) * wstride, m = smb + L_RING + slot_off(c) + p * 4096 + half * 1024;
    dma(wr, wvo0, so, m); dma(wr, wvo1, so + 8 * wstride, m + 1024);
  };
  unsigned tch;
  {
    const rsrc_t xr = mkr(x + (size_t)(tb * 64) * 1024, 65536);
    auto xrow = [&](int j) {
      const unsigned blk = (hg + j) & 7, r = blk * 8 + w;
      dma(xr, (lane * 16) ^ ((r & 15) << 4), r * 1024, smb + L_X0 + r * 1024 + (blk >> 2) * (L_X1 - L_X0 - 32768));
    };
    unsigned touched = 0;
    auto touch = [&]() {
      const int i = blockIdx.x >> 3, seg = (i & 1) * 2 + h, row = (((i >> 1) + 2 * (tb >> 2)) & 15) * 32 + l31;
      const char* a = seg == 0 ? wq + row * 1024 : wqkv + (seg - 1) * 1024 + row * 3072;
      a += ((3 - ((size_t)a >> 7)) & 7) * 128;
      if (i >= TOUCH_MIN) asm volatile("global_load_dword %0, %1, off" : "=v"(touched) : "v"(a) : "memory");
    };
    xrow(0);
    issue_share(0);
    if (w == 6) touch();
    if (w == 7) {
      u64 pc; unsigned len;
      asm volatile("s_getpc_b64 %0\nfk_code_0:\n\ts_mov_b32 %1, fk_code_1-fk_code_0" : "=s"(pc), "=s"(len));
      dma(mkr((const void*)pc, len), lane * 16, (blockIdx.x >> 3) * 1024, smb + L_GATE + 2048);
    }
    asm volatile("s_barrier" ::: "memory");
    {
      const rsrc_t r = mkr(hg < 4 ? wk : wv, 16384);
      const unsigned o = ((w + 2 * tb + hg) & 7) * 2048;
      dma(r, (lane * 16) ^ ((g & 1) << 7), o, smb + L_WKV + o); dma(r, (lane * 16) ^ ((g & 1) << 7), o + 1024, smb + L_WKV + o + 1024);
    }
    if (w == 2) dma(mkr(gate, 1024), lane * 16, 0, smb + L_GATE);
    if (w == 3) dma_dev(wsr, lane * 16, WS_EPOCH, smb + L_GATE + 1024);
#pragma unroll
    for (int j = 1; j < 8; ++j) xrow(j);
    issue_share(1);
    tch = touched;
  }
  const unsigned xb16 = l15 * 1024 + ((g ^ l15) << 4);
  const unsigned obo = WS_OB + (unsigned)((tb * 2 + (w >> 2)) * 32 + hg * 4 + p) * 1024 + lane * 16;
  u32x2 tag;
  f32x16 accl, acch;
  {
    const unsigned xb0 = smb + L_X0 + xb16, xb1 = xb0 + (L_X1 - L_X0);
    const unsigned wa = smb + L_RING + (w >> 2) * 8192 + (p >> 1) * 256 + g * 1024 + (l15 >> 2) * 64 + (((g ^ p) & 1) * 32) + (lane & 3) * 8;
    const unsigned cx = smb + ((hg & 2) ? L_X1 : L_X0) + (hg & 1) * 16384 + xb16 + (w & 1) * 512;
    const unsigned cw = w < 2 ? smb + L_WKV + w * 8192 + (8 * g + (l15 >> 2)) * 32 + (lane & 3) * 8 + (g & 1) * 128 : smb + L_GATE + (w & 1) * 512 + g * 16;
    const int kt = 2 * tb + ((hg >> 1) & 1);
    const unsigned tile = WS_KV + ((kt & 31) >> 3) * 32768 + (kt >> 5) * 8192 + (kt & 7) * 1024;
    const unsigned kvo = w == 0 ? tile + (hg < 4 ? (16 * (hg & 1) + l15 + 32 * (g >> 1)) * 16 + (g & 1) * 8 : 16384 + (hg & 1) * 512 + (g >> 1) * 256 + l15 * 16 + (g & 1) * 8)
                                : (w == 2 && hg >= 4 && lane < 16) ? WS_G + (tb * 64 + (hg & 3) * 16 + lane) * 8 : OOB;
    const unsigned role = w < 2 ? (hg >> 2) : (w < 4 && hg >= 4) ? 2 : 3;
    unsigned sk = rot << 6, x0 = xb0 ^ sk, x1 = xb1 ^ sk, so = ((unsigned)((2 + rot) & 15) * 32u + half * 8) * wstride, t0, tl, th;
    u64 c1, c2, modd, mw0, mw1, mlook;
    asm volatile(
        "s_cmp_eq_u32 %[w], 0\n\t"
        "s_cselect_b64 %[mw0], -1, 0\n\t"
        "s_cmp_eq_u32 %[w], 1\n\t"
        "s_cselect_b64 %[mw1], -1, 0\n\t"
        "s_cselect_b64 %[mlook], -1, 1\n\t"
        "s_and_b32 %[t0], %[w], 5\n\t"
        "s_cmp_eq_u32 %[t0], 1\n\t"
        "s_cselect_b64 %[modd], -1, 0\n\t"
        "s_cmp_eq_u32 %[role], 1\n\t"
        "s_cbranch_scc1 LPV%=\n\t"
        "s_cmp_eq_u32 %[role], 2\n\t"
        "s_cbranch_scc1 LPG%=\n\t"
        "s_cmp_eq_u32 %[role], 3\n\t"
        "s_cbranch_scc1 LPN%=\n\t"
        "s_waitcnt vmcnt(2)\n\t"
        "s_barrier\n\t"
        "ds_read_b128 v[80:83], %[cx0] offset:0\n\t"
        "ds_read_b64_tr_b16 v[84:85], %[cw0] offset:0\n\t"
        "ds_read_b64_tr_b16 v[86:87], %[cw1] offset:0\n\t"
        "ds_read_b128 v[88:91], %[cx1] offset:0\n\t"
        "ds_read_b64_tr_b16 v[92:93], %[cw0] offset:1024\n\t"
        "ds_read_b64_tr_b16 v[94:95], %[cw1] offset:1024\n\t"
        "ds_read_b128 v[96:99], %[cx2] offset:0\n\t"
        "ds_read_b64_tr_b16 v[100:101], %[cw0] offset:2048\n\t"
        "ds_read_b64_tr_b16 v[102:103], %[cw1] offset:2048\n\t"
        "ds_read_b128 v[104:107], %[cx3] offset:0\n\t"
        "ds_read_b64_tr_b16 v[108:109], %[cw0] offset:3072\n\t"
        "ds_read_b64_tr_b16 v[110:111], %[cw1] offset:3072\n\t"
        "ds_read_b128 v[112:115], %[cx0] offset:256\n\t"
        "ds_read_b64_tr_b16 v[116:117], %[cw0] offset:4096\n\t"
        "ds_read_b64_tr_b16 v[118:119], %[cw1] offset:4096\n\t"
        "s_waitcnt lgkmcnt(12)\n\t"
        "v_mfma_f32_16x16x32_bf16 v[120:123], v[84:87], v[80:83], 0\n\t"
        "ds_read_b128 v[80:83], %[cx1] offset:256\n\t"
        "ds_read_b64_tr_b16 v[84:85], %[cw0] offset:5120\n\t"
        "ds_read_b64_tr_b16 v[86:87], %[cw1] offset:5120\n\t"
        "s_waitcnt lgkmcnt(12)\n\t"
        "v_mfma_f32_16x16x32_bf16 v[120:123], v[92:95], v[88:91], v[120:123]\n\t"
        "ds_read_b128 v[88:91], %[cx2] offset:256\n\t"
        "ds_read_b64_tr_b16 v[92:93], %[cw0] offset:6144\n\t"
        "ds_read_b64_tr_b16 v[94:95], %[cw1] offset:6144\n\t"
        "s_waitcnt lgkmcnt(12)\n\t"
        "v_mfma_f32_16x16x32_bf16 v[120:123], v[100:103], v[96:99], v[120:123]\n\t"
        "ds_read_b128 v[96:99], %[cx3] offset:256\n\t"
        "ds_read_b64_tr_b16 v[100:101], %[cw0] offset:7168\n\t"
        "ds_read_b64_tr_b16 v[102:103], %[cw1] offset:7168\n\t"
        "s_waitcnt lgkmcnt(12)\n\t"
        "v_mfma_f32_16x16x32_bf16 v[120:123], v[108:111], v[104:107], v[120:123]\n\t"
        "s_waitcnt lgkmcnt(9)\n\t"
        "v_mfma_f32_16x16x32_bf16 v[120:123], v[116:119], v[112:115], v[120:123]\n\t"
        "s_waitcnt lgkmcnt(6)\n\t"
        "v_mfma_f32_16x16x32_bf16 v[120:123], v[84:87], v[80:83], v[120:123]\n\t"
        "s_waitcnt lgkmcnt(3)\n\t"
        "v_mfma_f32_16x16x32_bf16 v[120:123], v[92:95], v[88:91], v[120:123]\n\t"
        "s_waitcnt lgkmcnt(0)\n\t"
        "v_mfma_f32_16x16x32_bf16 v[120:123], v[100:103], v[96:99], v[120:123]\n\t"
        "s_branch LPC%=\n\t"
        "LPV%=:\n\t"
        "s_waitcnt vmcnt(2)\n\t"
        "s_barrier\n\t"
        "ds_read_b128 v[80:83], %[cx0] offset:0\n\t"
        "ds_read_b64_tr_b16 v[84:85], %[cw0] offset:0\n\t"
        "ds_read_b64_tr_b16 v[86:87], %[cw1] offset:0\n\t"
        "ds_read_b128 v[88:91], %[cx1] offset:0\n\t"
        "ds_read_b64_tr_b16 v[92:93], %[cw0] offset:1024\n\t"
        "ds_read_b64_tr_b16 v[94:95], %[cw1] offset:1024\n\t"
        "ds_read_b128 v[96:99], %[cx2] offset:0\n\t"
        "ds_read_b64_tr_b16 v[100:101], %[cw0] offset:2048\n\t"
        "ds_read_b64_tr_b16 v[102:103], %[cw1] offset:2048\n\t"
        "ds_read_b128 v[104:107], %[cx3] offset:0\n\t"
        "ds_read_b64_tr_b16 v[108:109], %[cw0] offset:3072\n\t"
        "ds_read_b64_tr_b16 v[110:111], %[cw1] offset:3072\n\t"
        "ds_read_b128 v[112:115], %[cx0] offset:256\n\t"
        "ds_read_b64_tr_b16 v[116:117], %[cw0] offset:4096\n\t"
        "ds_read_b64_tr_b16 v[118:119], %[cw1] offset:4096\n\t"
        "s_waitcnt lgkmcnt(12)\n\t"
        "v_mfma_f32_16x16x32_bf16 v[120:123], v[80:83], v[84:87], 0\n\t"
        "ds_read_b128 v[80:83], %[cx1] offset:256\n\t"
        "ds_read_b64_tr_b16 v[84:85], %[cw0] offset:5120\n\t"
        "ds_read_b64_tr_b16 v[86:87], %[cw1] offset:5120\n\t"
        "s_waitcnt lgkmcnt(12)\n\t"
        "v_mfma_f32_16x16x32_bf16 v[120:123], v[88:91], v[92:95], v[120:123]\n\t"
        "ds_read_b128 v[88:91], %[cx2] offset:256\n\t"
        "ds_read_b64_tr_b16 v[92:93], %[cw0] offset:6144\n\t"
        "ds_read_b64_tr_b16 v[94:95], %[cw1] offset:6144\n\t"
        "s_waitcnt lgkmcnt(12)\n\t"
        "v_mfma_f32_16x16x32_bf16 v[120:123], v[96:99], v[100:103], v[120:123]\n\t"
        "ds_read_b128 v[96:99], %[cx3] offset:256\n\t"
        "ds_read_b64_tr_b16 v[100:101], %[cw0] offset:7168\n\t"
        "ds_read_b64_tr_b16 v[102:103], %[cw1] offset:7168\n\t"
        "s_waitcnt lgkmcnt(12)\n\t"
        "v_mfma_f32_16x16x32_bf16 v[120:123], v[104:107], v[108:111], v[120:123]\n\t"
        "s_waitcnt lgkmcnt(9)\n\t"
        "v_mfma_f32_16x16x32_bf16 v[120:123], v[112:115], v[116:119], v[120:123]\n\t"
        "s_waitcnt lgkmcnt(6)\n\t"
        "v_mfma_f32_16x16x32_bf16 v[120:123], v[80:83], v[84:87], v[120:123]\n\t"
        "s_waitcnt lgkmcnt(3)\n\t"
        "v_mfma_f32_16x16x32_bf16 v[120:123], v[88:91], v[92:95], v[120:123]\n\t"
        "s_waitcnt lgkmcnt(0)\n\t"
        "v_mfma_f32_16x16x32_bf16 v[120:123], v[96:99], v[100:103], v[120:123]\n\t"
        "s_branch LPC%=\n\t"
        "LPN%=:\n\t"
        "s_waitcnt vmcnt(2)\n\t"
        "s_barrier\n\t"
        "s_branch LPC%=\n\t"
        "LPG%=:\n\t"
        "s_waitcnt vmcnt(2)\n\t"
        "s_barrier\n\t"
        "ds_read_b128 v[80:83], %[cx0] offset:0\n\t"
        "ds_read_b128 v[84:87], %[cw0] offset:0\n\t"
        "ds_read_b128 v[88:91], %[cx1] offset:0\n\t"
        "ds_read_b128 v[92:95], %[cw0] offset:64\n\t"
        "ds_read_b128 v[96:99], %[cx2] offset:0\n\t"
        "ds_read_b128 v[100:103], %[cw0] offset:128\n\t"
        "ds_read_b128 v[104:107], %[cx3] offset:0\n\t"
        "ds_read_b128 v[108:111], %[cw0] offset:192\n\t"
        "ds_read_b128 v[112:115], %[cx0] offset:256\n\t"
        "ds_read_b128 v[116:119], %[cw0] offset:256\n\t"
        "s_waitcnt lgkmcnt(8)\n\t"
        "v_mfma_f32_16x16x32_bf16 v[120:123], v[84:87], v[80:83], 0\n\t"
        "ds_read_b128 v[80:83], %[cx1] offset:256\n\t"
        "ds_read_b128 v[84:87], %[cw0] offset:320\n\t"
        "s_waitcnt lgkmcnt(8)\n\t"
        "v_mfma_f32_16x16x32_bf16 v[120:123], v[92:95], v[88:91], v[120:123]\n\t"
        "ds_read_b128 v[88:91], %[cx2] offset:256\n\t"
        "ds_read_b128 v[92:95], %[cw0] offset:384\n\t"
        "s_waitcnt lgkmcnt(8)\n\t"
        "v_mfma_f32_16x16x32_bf16 v[120:123], v[100:103], v[96:99], v[120:123]\n\t"
        "ds_read_b128 v[96:99], %[cx3] offset:256\n\t"
        "ds_read_b128 v[100:103], %[cw0] offset:448\n\t"
        "s_waitcnt lgkmcnt(8)\n\t"
        "v_mfma_f32_16x16x32_bf16 v[120:123], v[108:111], v[104:107], v[120:123]\n\t"
        "s_waitcnt lgkmcnt(6)\n\t"
        "v_mfma_f32_16x16x32_bf16 v[120:123], v[116:119], v[112:115], v[120:123]\n\t"
        "s_waitcnt lgkmcnt(4)\n\t"
        "v_mfma_f32_16x16x32_bf16 v[120:123], v[84:87], v[80:83], v[120:123]\n\t"
        "s_waitcnt lgkmcnt(2)\n\t"
        "v_mfma_f32_16x16x32_bf16 v[120:123], v[92:95], v[88:91], v[120:123]\n\t"
        "s_waitcnt lgkmcnt(0)\n\t"
        "v_mfma_f32_16x16x32_bf16 v[120:123], v[100:103], v[96:99], v[120:123]\n\t"
        "LPC%=:\n\t"
        "ds_read_b64 v[124:125], %[ea]\n\t"
        "ds_read_b64_tr_b16 v[48:49], %[wa] offset:32768\n\t"
        "ds_read_b64_tr_b16 v[50:51], %[wa] offset:33280\n\t"
        "ds_read_b128 v[32:35], %[x0] offset:0\n\t"
        "ds_read_b64_tr_b16 v[52:53], %[wa] offset:36864\n\t"
        "ds_read_b64_tr_b16 v[54:55], %[wa] offset:37376\n\t"
        "ds_read_b128 v[36:39], %[x0] offset:16384\n\t"
        "ds_read_b128 v[40:43], %[x1] offset:0\n\t"
        "ds_read_b128 v[44:47], %[x1] offset:16384\n\t"
        "s_add_u32 %[sk], %[sk], 64\n\t"
        "s_and_b32 %[sk], %[sk], 0x3c0\n\t"
        "v_xor_b32 %[x0], %[sk], %[xb0]\n\t"
        "v_xor_b32 %[x1], %[sk], %[xb1]\n\t"
        "s_mov_b64 exec, %[modd]\n\t"
        "ds_write_b128 %[pa], v[120:123]\n\t"
        "s_mov_b64 exec, -1\n\t"
        "s_add_u32 m0, %[sm], 0\n\t"
        "s_add_u32 %[t0], %[so], %[st8]\n\t"
        "buffer_load_dwordx4 %[v0], %[wr], %[so] offen lds\n\t"
        "s_add_u32 m0, m0, 0x400\n\t"
        "s_nop 0\n\t"
        "buffer_load_dwordx4 %[v1], %[wr], %[t0] offen lds\n\t"
        "s_add_u32 %[so], %[so], %[st32]\n\t"
        "s_cmp_ge_u32 %[so], %[lim]\n\t"
        "s_cselect_b32 %[t0], %[lim], 0\n\t"
        "s_sub_u32 %[so], %[so], %[t0]\n\t"
        "s_waitcnt lgkmcnt(8)\n\t"
        "v_readfirstlane_b32 %[tl], v124\n\t"
        "v_readfirstlane_b32 %[th], v125\n\t"
        "s_add_u32 m0, %[sm], 16384\n\t"
        "s_add_u32 %[t0], %[so], %[st8]\n\t"
        "buffer_load_dwordx4 %[v0], %[wr], %[so] offen lds\n\t"
        "s_add_u32 m0, m0, 0x400\n\t"
        "s_nop 0\n\t"
        "buffer_load_dwordx4 %[v1], %[wr], %[t0] offen lds\n\t"
        "s_add_u32 %[so], %[so], %[st32]\n\t"
        "s_cmp_ge_u32 %[so], %[lim]\n\t"
        "s_cselect_b32 %[t0], %[lim], 0\n\t"
        "s_sub_u32 %[so], %[so], %[t0]\n\t"
        "s_add_u32 %[tl], %[tl], 1\n\t"
        "s_addc_u32 %[th], %[th], 0\n\t"
        "v_mov_b32 v128, %[tl]\n\t"
        "v_mov_b32 v129, %[th]\n\t"
        "s_waitcnt vmcnt(4) lgkmcnt(0)\n\t"
        "s_barrier\n\t"
        "v_mfma_f32_16x16x32_bf16 v[166:169], v[32:35], v[48:51], 0\n\t"
        "ds_read_b128 v[124:127], %[pa]\n\t"
        "ds_read_b64_tr_b16 v[72:73], %[wa] offset:49152\n\t"
        "v_mfma_f32_16x16x32_bf16 v[170:173], v[32:35], v[52:55], 0\n\t"
        "ds_read_b128 v[56:59], %[x0] offset:0\n\t"
        "v_mfma_f32_16x16x32_bf16 v[174:177], v[36:39], v[48:51], 0\n\t"
        "ds_read_b64_tr_b16 v[74:75], %[wa] offset:49664\n\t"
        "ds_read_b64_tr_b16 v[76:77], %[wa] offset:53248\n\t"
        "v_mfma_f32_16x16x32_bf16 v[178:181], v[36:39], v[52:55], 0\n\t"
        "s_waitcnt lgkmcnt(4)\n\t"
        "v_add_f32 v120, v120, v124\n\t"
        "v_add_f32 v121, v121, v125\n\t"
        "v_add_f32 v122, v122, v126\n\t"
        "v_add_f32 v123, v123, v127\n\t"
        "ds_read_b128 v[60:63], %[x0] offset:16384\n\t"
        "v_mfma_f32_16x16x32_bf16 v[182:185], v[40:43], v[48:51], 0\n\t"
        "ds_read_b64_tr_b16 v[78:79], %[wa] offset:53760\n\t"
        "s_add_u32 m0, %[sm], 32768\n\t"
        "s_add_u32 %[t0], %[so], %[st8]\n\t"
        "buffer_load_dwordx4 %[v0], %[wr], %[so] offen lds\n\t"
        "v_mul_f32 v124, 0xbfb8aa3b, v120\n\t"
        "v_cvt_pk_bf16_f32 v126, v120, v121\n\t"
        "v_exp_f32 v124, v124\n\t"
        "v_cvt_pk_bf16_f32 v127, v122, v123\n\t"
        "v_add_f32 v124, 1.0, v124\n\t"
        "v_mfma_f32_16x16x32_bf16 v[186:189], v[40:43], v[52:55], 0\n\t"
        "ds_read_b128 v[64:67], %[x1] offset:0\n\t"
        "v_mfma_f32_16x16x32_bf16 v[190:193], v[44:47], v[48:51], 0\n\t"
        "ds_read_b128 v[68:71], %[x1] offset:16384\n\t"
        "v_rcp_f32 v124, v124\n\t"
        "s_nop 0\n\t"
        "v_cndmask_b32 v126, v124, v126, %[mw0]\n\t"
        "s_add_u32 m0, m0, 0x400\n\t"
        "s_nop 0\n\t"
        "buffer_load_dwordx4 %[v1], %[wr], %[t0] offen lds\n\t"
        "s_add_u32 %[so], %[so], %[st32]\n\t"
        "s_cmp_ge_u32 %[so], %[lim]\n\t"
        "s_cselect_b32 %[t0], %[lim], 0\n\t"
        "s_sub_u32 %[so], %[so], %[t0]\n\t"
        "s_add_u32 %[sk], %[sk], 64\n\t"
        "s_and_b32 %[sk], %[sk], 0x3c0\n\t"
        "v_xor_b32 %[x0], %[sk], %[xb0]\n\t"
        "v_xor_b32 %[x1], %[sk], %[xb1]\n\t"
        "v_mfma_f32_16x16x32_bf16 v[194:197], v[44:47], v[52:55], 0\n\t"
        "buffer_store_dwordx2 v[126:127], %[kvo], %[ws], 0 offen sc1\n\t"
        "s_waitcnt vmcnt(5) lgkmcnt(0)\n\t"
        "s_barrier\n\t"
        "v_mfma_f32_16x16x32_bf16 v[166:169], v[56:59], v[72:75], v[166:169]\n\t"
        "ds_read_b64_tr_b16 v[48:49], %[wa] offset:0\n\t"
        "v_mfma_f32_16x16x32_bf16 v[170:173], v[56:59], v[76:79], v[170:173]\n\t"
        "ds_read_b128 v[32:35], %[x0] offset:0\n\t"
        "v_mfma_f32_16x16x32_bf16 v[174:177], v[60:63], v[72:75], v[174:177]\n\t"
        "ds_read_b64_tr_b16 v[50:51], %[wa] offset:512\n\t"
        "ds_read_b64_tr_b16 v[52:53], %[wa] offset:4096\n\t"
        "v_mfma_f32_16x16x32_bf16 v[178:181], v[60:63], v[76:79], v[178:181]\n\t"
        "ds_read_b128 v[36:39], %[x0] offset:16384\n\t"
        "v_mfma_f32_16x16x32_bf16 v[182:185], v[64:67], v[72:75], v[182:185]\n\t"
        "ds_read_b64_tr_b16 v[54:55], %[wa] offset:4608\n\t"
        "s_add_u32 m0, %[sm], 49152\n\t"
        "s_add_u32 %[t0], %[so], %[st8]\n\t"
        "buffer_load_dwordx4 %[v0], %[wr], %[so] offen lds\n\t"
        "v_mfma_f32_16x16x32_bf16 v[186:189], v[64:67], v[76:79], v[186:189]\n\t"
        "ds_read_b128 v[40:43], %[x1] offset:0\n\t"
        "v_mfma_f32_16x16x32_bf16 v[190:193], v[68:71], v[72:75], v[190:193]\n\t"
        "ds_read_b128 v[44:47], %[x1] offset:16384\n\t"
        "s_add_u32 m0, m0, 0x400\n\t"
        "s_nop 0\n\t"
        "buffer_load_dwordx4 %[v1], %[wr], %[t0] offen lds\n\t"
        "s_add_u32 %[so], %[so], %[st32]\n\t"
        "s_cmp_ge_u32 %[so], %[lim]\n\t"
        "s_cselect_b32 %[t0], %[lim], 0\n\t"
        "s_sub_u32 %[so], %[so], %[t0]\n\t"
        "s_add_u32 %[sk], %[sk], 64\n\t"
        "s_and_b32 %[sk], %[sk], 0x3c0\n\t"
        "v_xor_b32 %[x0], %[sk], %[xb0]\n\t"
        "v_xor_b32 %[x1], %[sk], %[xb1]\n\t"
        "v_mfma_f32_16x16x32_bf16 v[194:197], v[68:71], v[76:79], v[194:197]\n\t"
        "s_waitcnt vmcnt(5) lgkmcnt(0)\n\t"
        "s_barrier\n\t"
        "v_mfma_f32_16x16x32_bf16 v[166:169], v[32:35], v[48:51], v[166:169]\n\t"
        "ds_read_b64_tr_b16 v[72:73], %[wa] offset:16384\n\t"
        "v_mfma_f32_16x16x32_bf16 v[170:173], v[32:35], v[52:55], v[170:173]\n\t"
        "ds_read_b128 v[56:59], %[x0] offset:0\n\t"
        "v_mfma_f32_16x16x32_bf16 v[174:177], v[36:39], v[48:51], v[174:177]\n\t"
        "ds_read_b64_tr_b16 v[74:75], %[wa] offset:16896\n\t"
        "ds_read_b64_tr_b16 v[76:77], %[wa] offset:20480\n\t"
        "v_mfma_f32_16x16x32_bf16 v[178:181], v[36:39], v[52:55], v[178:181]\n\t"
        "ds_read_b128 v[60:63], %[x0] offset:16384\n\t"
        "v_mfma_f32_16x16x32_bf16 v[182:185], v[40:43], v[48:51], v[182:185]\n\t"
        "ds_read_b64_tr_b16 v[78:79], %[wa] offset:20992\n\t"
        "s_add_u32 m0, %[sm], 0\n\t"
        "s_add_u32 %[t0], %[so], %[st8]\n\t"
        "buffer_load_dwordx4 %[v0], %[wr], %[so] offen lds\n\t"
        "v_mfma_f32_16x16x32_bf16 v[186:189], v[40:43], v[52:55], v[186:189]\n\t"
        "ds_read_b128 v[64:67], %[x1] offset:0\n\t"
        "v_mfma_f32_16x16x32_bf16 v[190:193], v[44:47], v[48:51], v[190:193]\n\t"
        "ds_read_b128 v[68:71], %[x1] offset:16384\n\t"
        "s_add_u32 m0, m0, 0x400\n\t"
        "s_nop 0\n\t"
        "buffer_load_dwordx4 %[v1], %[wr], %[t0] offen lds\n\t"
        "s_add_u32 %[so], %[so], %[st32]\n\t"
        "s_cmp_ge_u32 %[so], %[lim]\n\t"
        "s_cselect_b32 %[t0], %[lim], 0\n\t"
        "s_sub_u32 %[so], %[so], %[t0]\n\t"
        "s_add_u32 %[sk], %[sk], 64\n\t"
        "s_and_b32 %[sk], %[sk], 0x3c0\n\t"
        "v_xor_b32 %[x0], %[sk], %[xb0]\n\t"
        "v_xor_b32 %[x1], %[sk], %[xb1]\n\t"
        "v_mfma_f32_16x16x32_bf16 v[194:197], v[44:47], v[52:55], v[194:197]\n\t"
        "s_waitcnt vmcnt(5) lgkmcnt(0)\n\t"
        "s_barrier\n\t"
        "v_mfma_f32_16x16x32_bf16 v[166:169], v[56:59], v[72:75], v[166:169]\n\t"
        "ds_read_b64_tr_b16 v[48:49], %[wa] offset:32768\n\t"
        "v_mfma_f32_16x16x32_bf16 v[170:173], v[56:59], v[76:79], v[170:173]\n\t"
        "ds_read_b128 v[32:35], %[x0] offset:0\n\t"
        "v_mfma_f32_16x16x32_bf16 v[174:177], v[60:63], v[72:75], v[174:177]\n\t"
        "ds_read_b64_tr_b16 v[50:51], %[wa] offset:33280\n\t"
        "ds_read_b64_tr_b16 v[52:53], %[wa] offset:36864\n\t"
        "v_mfma_f32_16x16x32_bf16 v[178:181], v[60:63], v[76:79], v[178:181]\n\t"
        "ds_read_b128 v[36:39], %[x0] offset:16384\n\t"
        "v_mfma_f32_16x16x32_bf16 v[182:185], v[64:67], v[72:75], v[182:185]\n\t"
        "ds_read_b64_tr_b16 v[54:55], %[wa] offset:37376\n\t"
        "s_add_u32 m0, %[sm], 16384\n\t"
        "s_add_u32 %[t0], %[so], %[st8]\n\t"
        "buffer_load_dwordx4 %[v0], %[wr], %[so] offen lds\n\t"
        "v_mfma_f32_16x16x32_bf16 v[186:189], v[64:67], v[76:79], v[186:189]\n\t"
        "ds_read_b128 v[40:43], %[x1] offset:0\n\t"
        "v_mfma_f32_16x16x32_bf16 v[190:193], v[68:71], v[72:75], v[190:193]\n\t"
        "ds_read_b128 v[44:47], %[x1] offset:16384\n\t"
        "s_add_u32 m0, m0, 0x400\n\t"
        "s_nop 0\n\t"
        "buffer_load_dwordx4 %[v1], %[wr], %[t0] offen lds\n\t"
        "s_add_u32 %[so], %[so], %[st32]\n\t"
        "s_cmp_ge_u32 %[so], %[lim]\n\t"
        "s_cselect_b32 %[t0], %[lim], 0\n\t"
        "s_sub_u32 %[so], %[so], %[t0]\n\t"
        "s_add_u32 %[sk], %[sk], 64\n\t"
        "s_and_b32 %[sk], %[sk], 0x3c0\n\t"
        "v_xor_b32 %[x0], %[sk], %[xb0]\n\t"
        "v_xor_b32 %[x1], %[sk], %[xb1]\n\t"
        "v_mfma_f32_16x16x32_bf16 v[194:197], v[68:71], v[76:79], v[194:197]\n\t"
        "s_waitcnt vmcnt(4) lgkmcnt(0)\n\t"
        "s_barrier\n\t"
        "v_mfma_f32_16x16x32_bf16 v[166:169], v[32:35], v[48:51], v[166:169]\n\t"
        "ds_read_b64_tr_b16 v[72:73], %[wa] offset:49152\n\t"
        "v_mfma_f32_16x16x32_bf16 v[170:173], v[32:35], v[52:55], v[170:173]\n\t"
        "ds_read_b128 v[56:59], %[x0] offset:0\n\t"
        "v_mfma_f32_16x16x32_bf16 v[174:177], v[36:39], v[48:51], v[174:177]\n\t"
        "ds_read_b64_tr_b16 v[74:75], %[wa] offset:49664\n\t"
        "ds_read_b64_tr_b16 v[76:77], %[wa] offset:53248\n\t"
        "v_mfma_f32_16x16x32_bf16 v[178:181], v[36:39], v[52:55], v[178:181]\n\t"
        "ds_read_b128 v[60:63], %[x0] offset:16384\n\t"
        "v_mfma_f32_16x16x32_bf16 v[182:185], v[40:43], v[48:51], v[182:185]\n\t"
        "ds_read_b64_tr_b16 v[78:79], %[wa] offset:53760\n\t"
        "s_add_u32 m0, %[sm], 32768\n\t"
        "s_add_u32 %[t0], %[so], %[st8]\n\t"
        "buffer_load_dwordx4 %[v0], %[wr], %[so] offen lds\n\t"
        "v_mfma_f32_16x16x32_bf16 v[186:189], v[40:43], v[52:55], v[186:189]\n\t"
        "ds_read_b128 v[64:67], %[x1] offset:0\n\t"
        "v_mfma_f32_16x16x32_bf16 v[190:193], v[44:47], v[48:51], v[190:193]\n\t"
        "ds_read_b128 v[68:71], %[x1] offset:16384\n\t"
        "s_add_u32 m0, m0, 0x400\n\t"
        "s_nop 0\n\t"
        "buffer_load_dwordx4 %[v1], %[wr], %[t0] offen lds\n\t"
        "s_add_u32 %[so], %[so], %[st32]\n\t"
        "s_cmp_ge_u32 %[so], %[lim]\n\t"
        "s_cselect_b32 %[t0], %[lim], 0\n\t"
        "s_sub_u32 %[so], %[so], %[t0]\n\t"
        "s_add_u32 %[sk], %[sk], 64\n\t"
        "s_and_b32 %[sk], %[sk], 0x3c0\n\t"
        "v_xor_b32 %[x0], %[sk], %[xb0]\n\t"
        "v_xor_b32 %[x1], %[sk], %[xb1]\n\t"
        "v_mfma_f32_16x16x32_bf16 v[194:197], v[44:47], v[52:55], v[194:197]\n\t"
        "s_mov_b64 exec, 1\n\t"
        "buffer_store_dwordx2 v[128:129], %[fo], %[ws], 0 offen sc1\n\t"
        "s_mov_b64 exec, -1\n\t"
        "s_waitcnt vmcnt(5) lgkmcnt(0)\n\t"
        "s_barrier\n\t"
        "v_mfma_f32_16x16x32_bf16 v[166:169], v[56:59], v[72:75], v[166:169]\n\t"
        "ds_read_b64_tr_b16 v[48:49], %[wa] offset:0\n\t"
        "v_mfma_f32_16x16x32_bf16 v[170:173], v[56:59], v[76:79], v[170:173]\n\t"
        "ds_read_b128 v[32:35], %[x0] offset:0\n\t"
        "v_mfma_f32_16x16x32_bf16 v[174:177], v[60:63], v[72:75], v[174:177]\n\t"
        "ds_read_b64_tr_b16 v[50:51], %[wa] offset:512\n\t"
        "ds_read_b64_tr_b16 v[52:53], %[wa] offset:4096\n\t"
        "v_mfma_f32_16x16x32_bf16 v[178:181], v[60:63], v[76:79], v[178:181]\n\t"
        "ds_read_b128 v[36:39], %[x0] offset:16384\n\t"
        "v_mfma_f32_16x16x32_bf16 v[182:185], v[64:67], v[72:75], v[182:185]\n\t"
        "ds_read_b64_tr_b16 v[54:55], %[wa] offset:4608\n\t"
        "s_add_u32 m0, %[sm], 49152\n\t"
        "s_add_u32 %[t0], %[so], %[st8]\n\t"
        "buffer_load_dwordx4 %[v0], %[wr], %[so] offen lds\n\t"
        "v_mfma_f32_16x16x32_bf16 v[186:189], v[64:67], v[76:79], v[186:189]\n\t"
        "ds_read_b128 v[40:43], %[x1] offset:0\n\t"
        "v_mfma_f32_16x16x32_bf16 v[190:193], v[68:71], v[72:75], v[190:193]\n\t"
        "ds_read_b128 v[44:47], %[x1] offset:16384\n\t"
        "s_add_u32 m0, m0, 0x400\n\t"
        "s_nop 0\n\t"
        "buffer_load_dwordx4 %[v1], %[wr], %[t0] offen lds\n\t"
        "s_add_u32 %[so], %[so], %[st32]\n\t"
        "s_cmp_ge_u32 %[so], %[lim]\n\t"
        "s_cselect_b32 %[t0], %[lim], 0\n\t"
        "s_sub_u32 %[so], %[so], %[t0]\n\t"
        "s_add_u32 %[sk], %[sk], 64\n\t"
        "s_and_b32 %[sk], %[sk], 0x3c0\n\t"
        "v_xor_b32 %[x0], %[sk], %[xb0]\n\t"
        "v_xor_b32 %[x1], %[sk], %[xb1]\n\t"
        "v_mfma_f32_16x16x32_bf16 v[194:197], v[68:71], v[76:79], v[194:197]\n\t"
        "s_waitcnt vmcnt(5) lgkmcnt(0)\n\t"
        "s_barrier\n\t"
        "v_mfma_f32_16x16x32_bf16 v[166:169], v[32:35], v[48:51], v[166:169]\n\t"
        "ds_read_b64_tr_b16 v[72:73], %[wa] offset:16384\n\t"
        "v_mfma_f32_16x16x32_bf16 v[170:173], v[32:35], v[52:55], v[170:173]\n\t"
        "ds_read_b128 v[56:59], %[x0] offset:0\n\t"
        "v_mfma_f32_16x16x32_bf16 v[174:177], v[36:39], v[48:51], v[174:177]\n\t"
        "ds_read_b64_tr_b16 v[74:75], %[wa] offset:16896\n\t"
        "ds_read_b64_tr_b16 v[76:77], %[wa] offset:20480\n\t"
        "v_mfma_f32_16x16x32_bf16 v[178:181], v[36:39], v[52:55], v[178:181]\n\t"
        "ds_read_b128 v[60:63], %[x0] offset:16384\n\t"
        "v_mfma_f32_16x16x32_bf16 v[182:185], v[40:43], v[48:51], v[182:185]\n\t"
        "ds_read_b64_tr_b16 v[78:79], %[wa] offset:20992\n\t"
        "s_add_u32 m0, %[sm], 0\n\t"
        "s_add_u32 %[t0], %[so], %[st8]\n\t"
        "buffer_load_dwordx4 %[v0], %[wr], %[so] offen lds\n\t"
        "v_mfma_f32_16x16x32_bf16 v[186:189], v[40:43], v[52:55], v[186:189]\n\t"
        "ds_read_b128 v[64:67], %[x1] offset:0\n\t"
        "v_mfma_f32_16x16x32_bf16 v[190:193], v[44:47], v[48:51], v[190:193]\n\t"
        "ds_read_b128 v[68:71], %[x1] offset:16384\n\t"
        "s_add_u32 m0, m0, 0x400\n\t"
        "s_nop 0\n\t"
        "buffer_load_dwordx4 %[v1], %[wr], %[t0] offen lds\n\t"
        "s_add_u32 %[so], %[so], %[st32]\n\t"
        "s_cmp_ge_u32 %[so], %[lim]\n\t"
        "s_cselect_b32 %[t0], %[lim], 0\n\t"
        "s_sub_u32 %[so], %[so], %[t0]\n\t"
        "s_add_u32 %[sk], %[sk], 64\n\t"
        "s_and_b32 %[sk], %[sk], 0x3c0\n\t"
        "v_xor_b32 %[x0], %[sk], %[xb0]\n\t"
        "v_xor_b32 %[x1], %[sk], %[xb1]\n\t"
        "v_mfma_f32_16x16x32_bf16 v[194:197], v[44:47], v[52:55], v[194:197]\n\t"
        "s_waitcnt vmcnt(5) lgkmcnt(0)\n\t"
        "s_barrier\n\t"
        "v_mfma_f32_16x16x32_bf16 v[166:169], v[56:59], v[72:75], v[166:169]\n\t"
        "ds_read_b64_tr_b16 v[48:49], %[wa] offset:32768\n\t"
        "v_mfma_f32_16x16x32_bf16 v[170:173], v[56:59], v[76:79], v[170:173]\n\t"
        "ds_read_b128 v[32:35], %[x0] offset:0\n\t"
        "v_mfma_f32_16x16x32_bf16 v[174:177], v[60:63], v[72:75], v[174:177]\n\t"
        "ds_read_b64_tr_b16 v[50:51], %[wa] offset:33280\n\t"
        "ds_read_b64_tr_b16 v[52:53], %[wa] offset:36864\n\t"
        "v_mfma_f32_16x16x32_bf16 v[178:181], v[60:63], v[76:79], v[178:181]\n\t"
        "ds_read_b128 v[36:39], %[x0] offset:16384\n\t"
        "v_mfma_f32_16x16x32_bf16 v[182:185], v[64:67], v[72:75], v[182:185]\n\t"
        "ds_read_b64_tr_b16 v[54:55], %[wa] offset:37376\n\t"
        "s_add_u32 m0, %[sm], 16384\n\t"
        "s_add_u32 %[t0], %[so], %[st8]\n\t"
        "buffer_load_dwordx4 %[v0], %[wr], %[so] offen lds\n\t"
        "v_mfma_f32_16x16x32_bf16 v[186:189], v[64:67], v[76:79], v[186:189]\n\t"
        "ds_read_b128 v[40:43], %[x1] offset:0\n\t"
        "v_mfma_f32_16x16x32_bf16 v[190:193], v[68:71], v[72:75], v[190:193]\n\t"
        "ds_read_b128 v[44:47], %[x1] offset:16384\n\t"
        "s_add_u32 m0, m0, 0x400\n\t"
        "s_nop 0\n\t"
        "buffer_load_dwordx4 %[v1], %[wr], %[t0] offen lds\n\t"
        "s_add_u32 %[so], %[so], %[st32]\n\t"
        "s_cmp_ge_u32 %[so], %[lim]\n\t"
        "s_cselect_b32 %[t0], %[lim], 0\n\t"
        "s_sub_u32 %[so], %[so], %[t0]\n\t"
        "s_add_u32 %[sk], %[sk], 64\n\t"
        "s_and_b32 %[sk], %[sk], 0x3c0\n\t"
        "v_xor_b32 %[x0], %[sk], %[xb0]\n\t"
        "v_xor_b32 %[x1], %[sk], %[xb1]\n\t"
        "v_mfma_f32_16x16x32_bf16 v[194:197], v[68:71], v[76:79], v[194:197]\n\t"
        "s_waitcnt vmcnt(4) lgkmcnt(0)\n\t"
        "s_barrier\n\t"
        "v_mfma_f32_16x16x32_bf16 v[166:169], v[32:35], v[48:51], v[166:169]\n\t"
        "ds_read_b64_tr_b16 v[72:73], %[wa] offset:49152\n\t"
        "v_mfma_f32_16x16x32_bf16 v[170:173], v[32:35], v[52:55], v[170:173]\n\t"
        "ds_read_b128 v[56:59], %[x0] offset:0\n\t"
        "v_mfma_f32_16x16x32_bf16 v[174:177], v[36:39], v[48:51], v[174:177]\n\t"
        "ds_read_b64_tr_b16 v[74:75], %[wa] offset:49664\n\t"
        "ds_read_b64_tr_b16 v[76:77], %[wa] offset:53248\n\t"
        "v_mfma_f32_16x16x32_bf16 v[178:181], v[36:39], v[52:55], v[178:181]\n\t"
        "ds_read_b128 v[60:63], %[x0] offset:16384\n\t"
        "v_mfma_f32_16x16x32_bf16 v[182:185], v[40:43], v[48:51], v[182:185]\n\t"
        "ds_read_b64_tr_b16 v[78:79], %[wa] offset:53760\n\t"
        "s_add_u32 m0, %[sm], 32768\n\t"
        "s_add_u32 %[t0], %[so], %[st8]\n\t"
        "buffer_load_dwordx4 %[v0], %[wr], %[so] offen lds\n\t"
        "v_mfma_f32_16x16x32_bf16 v[186:189], v[40:43], v[52:55], v[186:189]\n\t"
        "ds_read_b128 v[64:67], %[x1] offset:0\n\t"
        "v_mfma_f32_16x16x32_bf16 v[190:193], v[44:47], v[48:51], v[190:193]\n\t"
        "ds_read_b128 v[68:71], %[x1] offset:16384\n\t"
        "s_add_u32 m0, m0, 0x400\n\t"
        "s_nop 0\n\t"
        "buffer_load_dwordx4 %[v1], %[wr], %[t0] offen lds\n\t"
        "s_add_u32 %[so], %[so], %[st32]\n\t"
        "s_cmp_ge_u32 %[so], %[lim]\n\t"
        "s_cselect_b32 %[t0], %[lim], 0\n\t"
        "s_sub_u32 %[so], %[so], %[t0]\n\t"
        "s_add_u32 %[sk], %[sk], 64\n\t"
        "s_and_b32 %[sk], %[sk], 0x3c0\n\t"
        "v_xor_b32 %[x0], %[sk], %[xb0]\n\t"
        "v_xor_b32 %[x1], %[sk], %[xb1]\n\t"
        "v_mfma_f32_16x16x32_bf16 v[194:197], v[44:47], v[52:55], v[194:197]\n\t"
        "s_mov_b64 exec, %[mlook]\n\t"
        "buffer_load_dwordx4 v[80:83], %[lo], %[ws], 0 offen sc1\n\t"
        "buffer_load_dwordx4 v[84:87], %[lo], %[ws], 0 offen offset:16 sc1\n\t"
        "s_mov_b64 exec, -1\n\t"
        "s_waitcnt vmcnt(6) lgkmcnt(0)\n\t"
        "s_barrier\n\t"
        "v_mfma_f32_16x16x32_bf16 v[166:169], v[56:59], v[72:75], v[166:169]\n\t"
        "ds_read_b64_tr_b16 v[48:49], %[wa] offset:0\n\t"
        "v_mfma_f32_16x16x32_bf16 v[170:173], v[56:59], v[76:79], v[170:173]\n\t"
        "ds_read_b128 v[32:35], %[x0] offset:0\n\t"
        "v_mfma_f32_16x16x32_bf16 v[174:177], v[60:63], v[72:75], v[174:177]\n\t"
        "ds_read_b64_tr_b16 v[50:51], %[wa] offset:512\n\t"
        "ds_read_b64_tr_b16 v[52:53], %[wa] offset:4096\n\t"
        "v_mfma_f32_16x16x32_bf16 v[178:181], v[60:63], v[76:79], v[178:181]\n\t"
        "ds_read_b128 v[36:39], %[x0] offset:16384\n\t"
        "v_mfma_f32_16x16x32_bf16 v[182:185], v[64:67], v[72:75], v[182:185]\n\t"
        "ds_read_b64_tr_b16 v[54:55], %[wa] offset:4608\n\t"
        "s_add_u32 m0, %[sm], 49152\n\t"
        "s_add_u32 %[t0], %[so], %[st8]\n\t"
        "buffer_load_dwordx4 %[v0], %[wr], %[so] offen lds\n\t"
        "v_mfma_f32_16x16x32_bf16 v[186:189], v[64:67], v[76:79], v[186:189]\n\t"
        "ds_read_b128 v[40:43], %[x1] offset:0\n\t"
        "v_mfma_f32_16x16x32_bf16 v[190:193], v[68:71], v[72:75], v[190:193]\n\t"
        "ds_read_b128 v[44:47], %[x1] offset:16384\n\t"
        "s_add_u32 m0, m0, 0x400\n\t"
        "s_nop 0\n\t"
        "buffer_load_dwordx4 %[v1], %[wr], %[t0] offen lds\n\t"
        "s_add_u32 %[so], %[so], %[st32]\n\t"
        "s_cmp_ge_u32 %[so], %[lim]\n\t"
        "s_cselect_b32 %[t0], %[lim], 0\n\t"
        "s_sub_u32 %[so], %[so], %[t0]\n\t"
        "s_add_u32 %[sk], %[sk], 64\n\t"
        "s_and_b32 %[sk], %[sk], 0x3c0\n\t"
        "v_xor_b32 %[x0], %[sk], %[xb0]\n\t"
        "v_xor_b32 %[x1], %[sk], %[xb1]\n\t"
        "v_mfma_f32_16x16x32_bf16 v[194:197], v[68:71], v[76:79], v[194:197]\n\t"
        "s_waitcnt vmcnt(6) lgkmcnt(0)\n\t"
        "s_barrier\n\t"
        "v_mfma_f32_16x16x32_bf16 v[166:169], v[32:35], v[48:51], v[166:169]\n\t"
        "ds_read_b64_tr_b16 v[72:73], %[wa] offset:16384\n\t"
        "v_mfma_f32_16x16x32_bf16 v[170:173], v[32:35], v[52:55], v[170:173]\n\t"
        "ds_read_b128 v[56:59], %[x0] offset:0\n\t"
        "v_mfma_f32_16x16x32_bf16 v[174:177], v[36:39], v[48:51], v[174:177]\n\t"
        "ds_read_b64_tr_b16 v[74:75], %[wa] offset:16896\n\t"
        "ds_read_b64_tr_b16 v[76:77], %[wa] offset:20480\n\t"
        "v_mfma_f32_16x16x32_bf16 v[178:181], v[36:39], v[52:55], v[178:181]\n\t"
        "ds_read_b128 v[60:63], %[x0] offset:16384\n\t"
        "v_mfma_f32_16x16x32_bf16 v[182:185], v[40:43], v[48:51], v[182:185]\n\t"
        "ds_read_b64_tr_b16 v[78:79], %[wa] offset:20992\n\t"
        "s_add_u32 m0, %[sm], 0\n\t"
        "s_add_u32 %[t0], %[so], %[st8]\n\t"
        "buffer_load_dwordx4 %[v0], %[wr], %[so] offen lds\n\t"
        "v_mfma_f32_16x16x32_bf16 v[186:189], v[40:43], v[52:55], v[186:189]\n\t"
        "ds_read_b128 v[64:67], %[x1] offset:0\n\t"
        "v_mfma_f32_16x16x32_bf16 v[190:193], v[44:47], v[48:51], v[190:193]\n\t"
        "ds_read_b128 v[68:71], %[x1] offset:16384\n\t"
        "s_add_u32 m0, m0, 0x400\n\t"
        "s_nop 0\n\t"
        "buffer_load_dwordx4 %[v1], %[wr], %[t0] offen lds\n\t"
        "s_add_u32 %[so], %[so], %[st32]\n\t"
        "s_cmp_ge_u32 %[so], %[lim]\n\t"
        "s_cselect_b32 %[t0], %[lim], 0\n\t"
        "s_sub_u32 %[so], %[so], %[t0]\n\t"
        "s_add_u32 %[sk], %[sk], 64\n\t"
        "s_and_b32 %[sk], %[sk], 0x3c0\n\t"
        "v_xor_b32 %[x0], %[sk], %[xb0]\n\t"
        "v_xor_b32 %[x1], %[sk], %[xb1]\n\t"
        "v_mfma_f32_16x16x32_bf16 v[194:197], v[44:47], v[52:55], v[194:197]\n\t"
        "s_waitcnt vmcnt(6) lgkmcnt(0)\n\t"
        "s_barrier\n\t"
        "v_mfma_f32_16x16x32_bf16 v[166:169], v[56:59], v[72:75], v[166:169]\n\t"
        "ds_read_b64_tr_b16 v[48:49], %[wa] offset:32768\n\t"
        "v_mfma_f32_16x16x32_bf16 v[170:173], v[56:59], v[76:79], v[170:173]\n\t"
        "ds_read_b128 v[32:35], %[x0] offset:0\n\t"
        "v_mfma_f32_16x16x32_bf16 v[174:177], v[60:63], v[72:75], v[174:177]\n\t"
        "ds_read_b64_tr_b16 v[50:51], %[wa] offset:33280\n\t"
        "ds_read_b64_tr_b16 v[52:53], %[wa] offset:36864\n\t"
        "v_mfma_f32_16x16x32_bf16 v[178:181], v[60:63], v[76:79], v[178:181]\n\t"
        "ds_read_b128 v[36:39], %[x0] offset:16384\n\t"
        "v_mfma_f32_16x16x32_bf16 v[182:185], v[64:67], v[72:75], v[182:185]\n\t"
        "ds_read_b64_tr_b16 v[54:55], %[wa] offset:37376\n\t"
        "s_add_u32 m0, %[sm], 16384\n\t"
        "s_add_u32 %[t0], %[so], %[st8]\n\t"
        "buffer_load_dwordx4 %[v0], %[wr], %[so] offen lds\n\t"
        "v_mfma_f32_16x16x32_bf16 v[186:189], v[64:67], v[76:79], v[186:189]\n\t"
        "ds_read_b128 v[40:43], %[x1] offset:0\n\t"
        "v_mfma_f32_16x16x32_bf16 v[190:193], v[68:71], v[72:75], v[190:193]\n\t"
        "ds_read_b128 v[44:47], %[x1] offset:16384\n\t"
        "s_add_u32 m0, m0, 0x400\n\t"
        "s_nop 0\n\t"
        "buffer_load_dwordx4 %[v1], %[wr], %[t0] offen lds\n\t"
        "s_add_u32 %[so], %[so], %[st32]\n\t"
        "s_cmp_ge_u32 %[so], %[lim]\n\t"
        "s_cselect_b32 %[t0], %[lim], 0\n\t"
        "s_sub_u32 %[so], %[so], %[t0]\n\t"
        "s_add_u32 %[sk], %[sk], 64\n\t"
        "s_and_b32 %[sk], %[sk], 0x3c0\n\t"
        "v_xor_b32 %[x0], %[sk], %[xb0]\n\t"
        "v_xor_b32 %[x1], %[sk], %[xb1]\n\t"
        "v_mfma_f32_16x16x32_bf16 v[194:197], v[68:71], v[76:79], v[194:197]\n\t"
        "s_waitcnt vmcnt(6)\n\t"
        "v_cmp_ne_u64 vcc, v[80:81], v[128:129]\n\t"
        "v_cmp_ne_u64 %[c1], v[82:83], v[128:129]\n\t"
        "v_cmp_ne_u64 %[c2], v[84:85], v[128:129]\n\t"
        "s_or_b64 vcc, vcc, %[c1]\n\t"
        "v_cmp_ne_u64 %[c1], v[86:87], v[128:129]\n\t"
        "s_or_b64 vcc, vcc, %[c2]\n\t"
        "s_or_b64 vcc, vcc, %[c1]\n\t"
        "s_and_b64 vcc, vcc, %[mw1]\n\t"
        "s_cbranch_vccnz LPS%=\n\t"
        "LPR%=:\n\t"
        "s_waitcnt vmcnt(4) lgkmcnt(0)\n\t"
        "s_barrier\n\t"
        "v_mfma_f32_16x16x32_bf16 v[166:169], v[32:35], v[48:51], v[166:169]\n\t"
        "ds_read_b64_tr_b16 v[72:73], %[wa] offset:49152\n\t"
        "v_mfma_f32_16x16x32_bf16 v[170:173], v[32:35], v[52:55], v[170:173]\n\t"
        "ds_read_b128 v[56:59], %[x0] offset:0\n\t"
        "v_mfma_f32_16x16x32_bf16 v[174:177], v[36:39], v[48:51], v[174:177]\n\t"
        "ds_read_b64_tr_b16 v[74:75], %[wa] offset:49664\n\t"
        "ds_read_b64_tr_b16 v[76:77], %[wa] offset:53248\n\t"
        "v_mfma_f32_16x16x32_bf16 v[178:181], v[36:39], v[52:55], v[178:181]\n\t"
        "ds_read_b128 v[60:63], %[x0] offset:16384\n\t"
        "v_mfma_f32_16x16x32_bf16 v[182:185], v[40:43], v[48:51], v[182:185]\n\t"
        "ds_read_b64_tr_b16 v[78:79], %[wa] offset:53760\n\t"
        "s_add_u32 m0, %[skv], 0\n\t"
        "s_add_u32 %[t0], %[skv], %[kvd]\n\t"
        "s_add_u32 %[t0], %[t0], 0\n\t"
        "buffer_load_dwordx4 %[vl], %[ws], %[t0] offen sc1 lds\n\t"
        "v_mfma_f32_16x16x32_bf16 v[186:189], v[40:43], v[52:55], v[186:189]\n\t"
        "ds_read_b128 v[64:67], %[x1] offset:0\n\t"
        "v_mfma_f32_16x16x32_bf16 v[190:193], v[44:47], v[48:51], v[190:193]\n\t"
        "ds_read_b128 v[68:71], %[x1] offset:16384\n\t"
        "buffer_load_dwordx4 %[vl], %[ws], %[t0] offen offset:1024 sc1 lds\n\t"
        "s_add_u32 %[sk], %[sk], 64\n\t"
        "s_and_b32 %[sk], %[sk], 0x3c0\n\t"
        "v_xor_b32 %[x0], %[sk], %[xb0]\n\t"
        "v_xor_b32 %[x1], %[sk], %[xb1]\n\t"
        "v_mfma_f32_16x16x32_bf16 v[194:197], v[44:47], v[52:55], v[194:197]\n\t"
        "s_waitcnt vmcnt(4) lgkmcnt(0)\n\t"
        "s_barrier\n\t"
        "v_mfma_f32_16x16x32_bf16 v[166:169], v[56:59], v[72:75], v[166:169]\n\t"
        "ds_read_b64_tr_b16 v[48:49], %[wa] offset:0\n\t"
        "v_mfma_f32_16x16x32_bf16 v[170:173], v[56:59], v[76:79], v[170:173]\n\t"
        "ds_read_b128 v[32:35], %[x0] offset:0\n\t"
        "v_mfma_f32_16x16x32_bf16 v[174:177], v[60:63], v[72:75], v[174:177]\n\t"
        "ds_read_b64_tr_b16 v[50:51], %[wa] offset:512\n\t"
        "ds_read_b64_tr_b16 v[52:53], %[wa] offset:4096\n\t"
        "v_mfma_f32_16x16x32_bf16 v[178:181], v[60:63], v[76:79], v[178:181]\n\t"
        "ds_read_b128 v[36:39], %[x0] offset:16384\n\t"
        "v_mfma_f32_16x16x32_bf16 v[182:185], v[64:67], v[72:75], v[182:185]\n\t"
        "ds_read_b64_tr_b16 v[54:55], %[wa] offset:4608\n\t"
        "s_add_u32 m0, %[skv], 2048\n\t"
        "s_add_u32 %[t0], %[skv], %[kvd]\n\t"
        "s_add_u32 %[t0], %[t0], 2048\n\t"
        "buffer_load_dwordx4 %[vl], %[ws], %[t0] offen sc1 lds\n\t"
        "v_mfma_f32_16x16x32_bf16 v[186:189], v[64:67], v[76:79], v[186:189]\n\t"
        "ds_read_b128 v[40:43], %[x1] offset:0\n\t"
        "v_mfma_f32_16x16x32_bf16 v[190:193], v[68:71], v[72:75], v[190:193]\n\t"
        "ds_read_b128 v[44:47], %[x1] offset:16384\n\t"
        "buffer_load_dwordx4 %[vl], %[ws], %[t0] offen offset:1024 sc1 lds\n\t"
        "s_add_u32 %[sk], %[sk], 64\n\t"
        "s_and_b32 %[sk], %[sk], 0x3c0\n\t"
        "v_xor_b32 %[x0], %[sk], %[xb0]\n\t"
        "v_xor_b32 %[x1], %[sk], %[xb1]\n\t"
        "v_mfma_f32_16x16x32_bf16 v[194:197], v[68:71], v[76:79], v[194:197]\n\t"
        "s_waitcnt vmcnt(4) lgkmcnt(0)\n\t"
        "s_barrier\n\t"
        "v_mfma_f32_16x16x32_bf16 v[166:169], v[32:35], v[48:51], v[166:169]\n\t"
        "ds_read_b64_tr_b16 v[72:73], %[wa] offset:16384\n\t"
        "v_mfma_f32_16x16x32_bf16 v[170:173], v[32:35], v[52:55], v[170:173]\n\t"
        "ds_read_b128 v[56:59], %[x0] offset:0\n\t"
        "v_mfma_f32_16x16x32_bf16 v[174:177], v[36:39], v[48:51], v[174:177]\n\t"
        "ds_read_b64_tr_b16 v[74:75], %[wa] offset:16896\n\t"
        "ds_read_b64_tr_b16 v[76:77], %[wa] offset:20480\n\t"
        "v_mfma_f32_16x16x32_bf16 v[178:181], v[36:39], v[52:55], v[178:181]\n\t"
        "ds_read_b128 v[60:63], %[x0] offset:16384\n\t"
        "v_mfma_f32_16x16x32_bf16 v[182:185], v[40:43], v[48:51], v[182:185]\n\t"
        "ds_read_b64_tr_b16 v[78:79], %[wa] offset:20992\n\t"
        "v_mfma_f32_16x16x32_bf16 v[186:189], v[40:43], v[52:55], v[186:189]\n\t"
        "ds_read_b128 v[64:67], %[x1] offset:0\n\t"
        "v_mfma_f32_16x16x32_bf16 v[190:193], v[44:47], v[48:51], v[190:193]\n\t"
        "ds_read_b128 v[68:71], %[x1] offset:16384\n\t"
        "v_mfma_f32_16x16x32_bf16 v[194:197], v[44:47], v[52:55], v[194:197]\n\t"
        "s_waitcnt lgkmcnt(0)\n\t"
        "v_mfma_f32_16x16x32_bf16 v[166:169], v[56:59], v[72:75], v[166:169]\n\t"
        "v_mfma_f32_16x16x32_bf16 v[174:177], v[60:63], v[72:75], v[174:177]\n\t"
        "v_mfma_f32_16x16x32_bf16 v[182:185], v[64:67], v[72:75], v[182:185]\n\t"
        "v_mfma_f32_16x16x32_bf16 v[190:193], v[68:71], v[72:75], v[190:193]\n\t"
        "v_mfma_f32_16x16x32_bf16 v[170:173], v[56:59], v[76:79], v[170:173]\n\t"
        "v_mfma_f32_16x16x32_bf16 v[178:181], v[60:63], v[76:79], v[178:181]\n\t"
        "v_mfma_f32_16x16x32_bf16 v[186:189], v[64:67], v[76:79], v[186:189]\n\t"
        "v_mfma_f32_16x16x32_bf16 v[194:197], v[68:71], v[76:79], v[194:197]\n\t"
        "s_branch LPE%=\n\t"
        "LPS%=:\n\t"
        "s_mov_b32 %[t0], 0x100000\n\t"
        "LPL%=:\n\t"
        "buffer_load_dwordx4 v[80:83], %[lo], %[ws], 0 offen sc1\n\t"
        "buffer_load_dwordx4 v[84:87], %[lo], %[ws], 0 offen offset:16 sc1\n\t"
        "s_waitcnt vmcnt(0)\n\t"
        "v_cmp_ne_u64 vcc, v[80:81], v[128:129]\n\t"
        "v_cmp_ne_u64 %[c1], v[82:83], v[128:129]\n\t"
        "v_cmp_ne_u64 %[c2], v[84:85], v[128:129]\n\t"
        "s_or_b64 vcc, vcc, %[c1]\n\t"
        "v_cmp_ne_u64 %[c1], v[86:87], v[128:129]\n\t"
        "s_or_b64 vcc, vcc, %[c2]\n\t"
        "s_or_b64 vcc, vcc, %[c1]\n\t"
        "s_cbranch_vccz LPR%=\n\t"
        "s_sleep 1\n\t"
        "s_sub_u32 %[t0], %[t0], 1\n\t"
        "s_cmp_lg_u32 %[t0], 0\n\t"
        "s_cbranch_scc1 LPL%=\n\t"
        "s_branch LPR%=\n\t"
        "LPE%=:\n\t"
        "s_nop 7\n\t"
        : "=&{v[166:181]}"(accl), "=&{v[182:197]}"(acch), [sk] "+s"(sk), [x0] "+v"(x0), [x1] "+v"(x1), [so] "+s"(so), [t0] "=&s"(t0), [tl] "=&s"(tl), [th] "=&s"(th),
          [c1] "=&s"(c1), [c2] "=&s"(c2), [modd] "=&s"(modd), [mw0] "=&s"(mw0), [mw1] "=&s"(mw1), [mlook] "=&s"(mlook)
        : [xb0] "v"(xb0), [xb1] "v"(xb1), [wa] "v"(wa), [v0] "v"(wvo0), [v1] "v"(wvo1), [vl] "v"(lane * 16u), [cx0] "v"(cx), [cx1] "v"(cx ^ 64), [cx2] "v"(cx ^ 128),
          [cx3] "v"(cx ^ 192), [cw0] "v"(cw), [cw1] "v"(cw ^ 128), [ea] "v"(smb + L_GATE + 1024), [pa] "v"(smb + L_GATE + 4096 + (p >> 1) * 1024 + lane * 16),
          [kvo] "v"(kvo), [fo] "v"(w == 0 ? kv_flag(tb) + hg * 8 : OOB), [lo] "v"(w == 1 ? kv_flag(lane >> 1) + (lane & 1) * 32 : OOB),
          [wr] "s"(wr), [ws] "s"(wsr), [role] "s"(role), [st8] "s"(8 * wstride), [st32] "s"(32 * wstride), [lim] "s"(512 * wstride),
          [sm] "s"(smb + L_RING + p * 4096 + half * 1024), [skv] "s"(smb + w * 4096), [kvd] "s"(WS_KV - smb), [w] "s"(w)
        : "v32", "v33", "v34", "v35", "v36", "v37", "v38", "v39", "v40", "v41", "v42", "v43", "v44", "v45", "v46", "v47", "v48", "v49", "v50", "v51", "v52", "v53", "v54", "v55", "v56", "v57", "v58", "v59", "v60", "v61", "v62", "v63", "v64", "v65", "v66", "v67", "v68", "v69", "v70", "v71", "v72", "v73", "v74", "v75", "v76", "v77", "v78", "v79", "v80", "v81", "v82", "v83", "v84", "v85", "v86", "v87", "v88", "v89", "v90", "v91", "v92", "v93", "v94", "v95", "v96", "v97", "v98", "v99", "v100", "v101", "v102", "v103", "v104", "v105", "v106", "v107", "v108", "v109", "v110", "v111", "v112", "v113", "v114", "v115", "v116", "v117", "v118", "v119", "v120", "v121", "v122", "v123", "v124", "v125", "v126", "v127", "v128", "v129", "vcc", "scc", "memory");
    tag = (u32x2){tl, th};
    asm volatile("" :: "v"(tch));
  }
  auto tile4 = [&](int i) { const f32x16& a = i < 4 ? accl : acch; const int o = 4 * (i & 3); return (f32x4){a[o], a[o + 1], a[o + 2], a[o + 3]}; };
  auto pk4 = [](f32x4 v) { return (u32x2){pk2(v[0], v[1]), pk2(v[2], v[3])}; };
  auto phi4 = [](f32x4 v) { return (f32x4){phi(v[0]), phi(v[1]), phi(v[2]), phi(v[3])}; };
  const unsigned imgw = (16 * p + l15) * RP + 8 * g;
  if (!loader) {
    const float qs = 0.25f * 1.4426950408889634f;
#pragma unroll
    for (int t = 0; t < 4; ++t) *(LAS u32x2*)(smb + L_SCR + imgw + t * 32) = pk4(tile4(2 * t) * qs);
  }
  WAIT_VM(0);
  BAR();
  const int hd = w & 3, kh = w >> 2;
  auto issue_kv = [&](int blk) {
    dma8_dev(wsr, lane * 16, WS_KV + blk * 32768 + (w - 4) * 8192, smb + blk * 32768 + (w - 4) * 8192);
  };
  const unsigned ovo = (unsigned)(4 * h + ((lane >> 2) & 3)) * 1024 + (g & 1) * 64 + (lane & 3) * 16;
  auto issue_wo = [&](int part) {
    const rsrc_t r = mkr((part < 2 ? woa : wob) + hg * 128, 511 * 1024 + 128);
    dma8s(r, ovo, ((part & 1) * 256 + (w - 4) * 64) * 1024, smb + part * 32768 + (w - 4) * 8192, 8192);
  };
  u32x16 wo3a, wo3b;
  const int xo = hg;
  u32x4 ofa[8];
  auto load_ofa = [&]() {
#pragma unroll
    for (int s = 0; s < 8; ++s) ofa[s] = __builtin_amdgcn_raw_buffer_load_b128(wsr, (unsigned)((tb * 2 + (w >> 2)) * 32 + 8 * (w & 3)) * 1024 + lane * 16, WS_OA + (s ^ xo) * 1024, 16);
  };
  const unsigned imgr = (16 * hd + 8 * h + (l15 >> 2)) * RP + (16 * (g & 1) + 4 * (lane & 3)) * 2;
  const unsigned qra = smb + L_SCR + imgr + (kh ^ 1) * 64, qrb = smb + L_SCR + imgr + kh * 64;
  const u32x4 qa = ldstr2(qra, qra + 4 * RP), qb = ldstr2(qrb, qrb + 4 * RP);
  if (loader) __builtin_amdgcn_s_setprio(1);
  __builtin_amdgcn_raw_buffer_store_b128((u32x4){~0u, ~0u, ~0u, ~0u}, wsr, obo, 0, 16);
  if (loader) {
    issue_kv(1);
    const u32x4 ones = {0x3f803f80u, 0x3f803f80u, 0x3f803f80u, 0x3f803f80u};
    f32x4 kv = {0, 0, 0, 0}, z = {0, 0, 0, 0};
#pragma unroll
    for (int j = 0; j < 2; ++j) {
      const u32x2 k0 = pk4(phi4(tile4(4 * j))), k1 = pk4(phi4(tile4(4 * j + 2))), v0 = pk4(tile4(4 * j + 1)), v1 = pk4(tile4(4 * j + 3));
      const u32x4 kf = {k0[0], k0[1], k1[0], k1[1]}, vf = {v0[0], v0[1], v1[0], v1[1]};
      kv = mf16(kf, vf, kv); z = mf16(kf, ones, z);
    }
    const unsigned go = WS_KVP + (tb * 8 + hg) * 4352 + hd * 1088 + g * 16;
    __builtin_amdgcn_raw_buffer_store_b128(__builtin_bit_cast(u32x4, kv), wsr, go + l15 * 64, 0, 16);
    if (l15 == 0) __builtin_amdgcn_raw_buffer_store_b128(__builtin_bit_cast(u32x4, z), wsr, go + 1024, 0, 16);
  }

  float ma, mb;
  {
    const u32x4 k0 = lds128(smb + lane * 16);
    f32x16 sa = mf32(k0, qa, zero16), sb = mf32(k0, qb, zero16);
    ma = sa[0]; mb = sb[0];
#pragma unroll
    for (int i = 1; i < 16; ++i) { ma = fmaxf(ma, sa[i]); mb = fmaxf(mb, sb[i]); }
    ma = xhalf_max(ma); mb = xhalf_max(mb);
  }
  float o[9], gt;
  {
    f32x16 na, nb, aa, ab, sa, sb;
    u32x8 kb; u32x16 vb;
    u32x16 bf0, bf1;
    const unsigned one = (l31 == 16 || l31 == 20) ? 0x3f803f80u : 0u;
    const u32x2 one2 = {one, one};
    const f32x2 na2 = {-ma, -ma}, nb2 = {-mb, -mb};
    unsigned ak = smb + kh * 8192 + lane * 16, av = smb + 16384 + kh * 8192 + h * 256 + l15 * 16;
    unsigned idx = 0xdddd;
    const u64 vm = 0x0000ffff0000ffffull;
#define BUF_IO "+{v[132:147]}"(bf0), "+{v[148:163]}"(bf1)
#define ATT_IO : "+{v[0:15]}"(sa), "+{v[16:31]}"(sb), "+{v[64:79]}"(aa), "+{v[80:95]}"(ab), "+{v[104:111]}"(kb), "+{v[112:127]}"(vb), "+{v129}"(ak), "+{v130}"(av), "+{v128}"(idx), BUF_IO \
               : "{v[32:47]}"(na), "{v[48:63]}"(nb), "{v[96:99]}"(qa), "{v[100:103]}"(qb), [vm] "s"(vm) : "memory"
    __builtin_amdgcn_s_setprio(0);
    asm volatile(
        "ds_read_b128 v[104:107], v129 offset:0\n\t"
        "ds_read_b128 v[108:111], v129 offset:1024\n\t"
        "v_mov_b64 v[112:113], %[one]\n\t"
        "v_mov_b64 v[114:115], %[one]\n\t"
        "v_mov_b64 v[116:117], %[one]\n\t"
        "v_mov_b64 v[118:119], %[one]\n\t"
        "v_mov_b64 v[120:121], %[one]\n\t"
        "v_mov_b64 v[122:123], %[one]\n\t"
        "v_mov_b64 v[124:125], %[one]\n\t"
        "v_mov_b64 v[126:127], %[one]\n\t"
        "s_mov_b64 exec, %[vm]\n\t"
        "ds_read_b128 v[112:115], v130 offset:0\n\t"
        "ds_read_b128 v[116:119], v130 offset:512\n\t"
        "s_mov_b64 exec, -1\n\t"
        "v_mov_b64 v[32:33], %[na]\n\t"
        "v_mov_b64 v[34:35], %[na]\n\t"
        "v_mov_b64 v[36:37], %[na]\n\t"
        "v_mov_b64 v[38:39], %[na]\n\t"
        "v_mov_b64 v[40:41], %[na]\n\t"
        "v_mov_b64 v[42:43], %[na]\n\t"
        "v_mov_b64 v[44:45], %[na]\n\t"
        "v_mov_b64 v[46:47], %[na]\n\t"
        "v_mov_b64 v[24:25], 0\n\t"
        "v_mov_b64 v[26:27], 0\n\t"
        "s_waitcnt lgkmcnt(3)\n\t"
        "v_mfma_f32_32x32x16_bf16 v[0:15], v[104:107], v[96:99], v[32:47]\n\t"
        "v_mov_b32 v28, 0xff800000\n\t"
        "v_mov_b32 v29, 0xff800000\n\t"
        "v_mov_b32 v30, 0xff800000\n\t"
        "v_mov_b32 v31, 0xff800000\n\t"
        "v_mov_b64 v[48:49], %[nb]\n\t"
        "v_mov_b64 v[50:51], %[nb]\n\t"
        "v_mov_b64 v[52:53], %[nb]\n\t"
        "v_mov_b64 v[54:55], %[nb]\n\t"
        "v_mov_b64 v[56:57], %[nb]\n\t"
        "v_mov_b64 v[58:59], %[nb]\n\t"
        "v_mov_b64 v[60:61], %[nb]\n\t"
        "v_mov_b64 v[62:63], %[nb]\n\t"
        "v_mov_b64 v[64:65], 0\n\t"
        "v_mov_b64 v[66:67], 0\n\t"
        "v_mov_b64 v[68:69], 0\n\t"
        "v_mov_b64 v[70:71], 0\n\t"
        "v_mov_b64 v[72:73], 0\n\t"
        "v_mov_b64 v[74:75], 0\n\t"
        "v_mov_b64 v[76:77], 0\n\t"
        "v_mov_b64 v[78:79], 0\n\t"
        "v_mov_b64 v[80:81], 0\n\t"
        "v_mov_b64 v[82:83], 0\n\t"
        "v_mov_b64 v[84:85], 0\n\t"
        "v_mov_b64 v[86:87], 0\n\t"
        "v_mov_b64 v[88:89], 0\n\t"
        "v_mov_b64 v[90:91], 0\n\t"
        "v_mov_b64 v[92:93], 0\n\t"
        "v_mov_b64 v[94:95], 0\n\t"
        "s_waitcnt lgkmcnt(0)\n\t"
        : "=&{v[0:15]}"(sa), "=&{v[16:31]}"(sb), "=&{v[104:111]}"(kb), "=&{v[112:127]}"(vb), "=&{v[32:47]}"(na), "=&{v[48:63]}"(nb),
          "=&{v[64:79]}"(aa), "=&{v[80:95]}"(ab), "=&{v[132:147]}"(bf0), "=&{v[148:163]}"(bf1)
        : "{v[96:99]}"(qa), "{v129}"(ak), "{v130}"(av), [vm] "s"(vm), [one] "v"(one2), [na] "v"(na2), [nb] "v"(nb2) : "memory");
#pragma unroll
    for (int it = 0; it < 4; ++it) {
      asm volatile(
        "v_exp_f32 v28, v28\n\t"
        "v_exp_f32 v29, v29\n\t"
        "v_exp_f32 v30, v30\n\t"
        "v_exp_f32 v31, v31\n\t"
        "v_exp_f32 v0, v0\n\t"
        "v_exp_f32 v1, v1\n\t"
        "v_smfmac_f32_32x32x32_bf16 v[80:95], v[124:127], v[24:31], v128\n\t"
        "s_mov_b64 exec, %[vm]\n\t"
        "ds_read_b128 v[120:123], v130 offset:1024\n\t"
        "ds_read_b128 v[124:127], v130 offset:1536\n\t"
        "s_mov_b64 exec, -1\n\t"
        "v_exp_f32 v2, v2\n\t"
        "v_exp_f32 v3, v3\n\t"
        "v_exp_f32 v4, v4\n\t"
        "v_exp_f32 v5, v5\n\t"
        "v_exp_f32 v6, v6\n\t"
        "v_mfma_f32_32x32x16_bf16 v[16:31], v[104:107], v[100:103], v[48:63]\n\t"
        "ds_read_b128 v[104:107], v129 offset:2048\n\t"
        "v_exp_f32 v7, v7\n\t"
        "v_exp_f32 v8, v8\n\t"
        "v_exp_f32 v9, v9\n\t"
        "v_exp_f32 v10, v10\n\t"
        "v_exp_f32 v11, v11\n\t"
        "v_smfmac_f32_32x32x32_bf16 v[64:79], v[112:115], v[0:7], v128\n\t"
        "v_exp_f32 v12, v12\n\t"
        "v_exp_f32 v13, v13\n\t"
        "v_exp_f32 v14, v14\n\t"
        "v_exp_f32 v15, v15\n\t"
        "v_exp_f32 v16, v16\n\t"
        "v_exp_f32 v17, v17\n\t"
        "v_smfmac_f32_32x32x32_bf16 v[64:79], v[116:119], v[8:15], v128\n\t"
        "v_exp_f32 v18, v18\n\t"
        "v_exp_f32 v19, v19\n\t"
        "v_exp_f32 v20, v20\n\t"
        "v_exp_f32 v21, v21\n\t"
        "v_mfma_f32_32x32x16_bf16 v[0:15], v[108:111], v[96:99], v[32:47]\n\t"
        "v_exp_f32 v22, v22\n\t"
        "v_exp_f32 v23, v23\n\t"
        "v_exp_f32 v24, v24\n\t"
        "v_exp_f32 v25, v25\n\t"
        "v_exp_f32 v26, v26\n\t"
        "v_exp_f32 v27, v27\n\t"
        "v_smfmac_f32_32x32x32_bf16 v[80:95], v[112:115], v[16:23], v128\n\t"
        "s_waitcnt lgkmcnt(0)\n\t"
        "v_exp_f32 v28, v28\n\t"
        "v_exp_f32 v29, v29\n\t"
        "v_exp_f32 v30, v30\n\t"
        "v_exp_f32 v31, v31\n\t"
        "v_exp_f32 v0, v0\n\t"
        "v_exp_f32 v1, v1\n\t"
        "v_smfmac_f32_32x32x32_bf16 v[80:95], v[116:119], v[24:31], v128\n\t"
        "s_mov_b64 exec, %[vm]\n\t"
        "ds_read_b128 v[112:115], v130 offset:2048\n\t"
        "ds_read_b128 v[116:119], v130 offset:2560\n\t"
        "s_mov_b64 exec, -1\n\t"
        "v_exp_f32 v2, v2\n\t"
        "v_exp_f32 v3, v3\n\t"
        "v_exp_f32 v4, v4\n\t"
        "v_exp_f32 v5, v5\n\t"
        "v_exp_f32 v6, v6\n\t"
        "v_mfma_f32_32x32x16_bf16 v[16:31], v[108:111], v[100:103], v[48:63]\n\t"
        "ds_read_b128 v[108:111], v129 offset:3072\n\t"
        "v_exp_f32 v7, v7\n\t"
        "v_exp_f32 v8, v8\n\t"
        "v_exp_f32 v9, v9\n\t"
        "v_exp_f32 v10, v10\n\t"
        "v_exp_f32 v11, v11\n\t"
        "v_smfmac_f32_32x32x32_bf16 v[64:79], v[120:123], v[0:7], v128\n\t"
        "v_exp_f32 v12, v12\n\t"
        "v_exp_f32 v13, v13\n\t"
        "v_exp_f32 v14, v14\n\t"
        "v_exp_f32 v15, v15\n\t"
        "v_exp_f32 v16, v16\n\t"
        "v_exp_f32 v17, v17\n\t"
        "v_smfmac_f32_32x32x32_bf16 v[64:79], v[124:127], v[8:15], v128\n\t"
        "v_exp_f32 v18, v18\n\t"
        "v_exp_f32 v19, v19\n\t"
        "v_exp_f32 v20, v20\n\t"
        "v_exp_f32 v21, v21\n\t"
        "v_mfma_f32_32x32x16_bf16 v[0:15], v[104:107], v[96:99], v[32:47]\n\t"
        "v_exp_f32 v22, v22\n\t"
        "v_exp_f32 v23, v23\n\t"
        "v_exp_f32 v24, v24\n\t"
        "v_exp_f32 v25, v25\n\t"
        "v_exp_f32 v26, v26\n\t"
        "v_exp_f32 v27, v27\n\t"
        "v_smfmac_f32_32x32x32_bf16 v[80:95], v[120:123], v[16:23], v128\n\t"
        "s_waitcnt lgkmcnt(0)\n\t"
        "v_exp_f32 v28, v28\n\t"
        "v_exp_f32 v29, v29\n\t"
        "v_exp_f32 v30, v30\n\t"
        "v_exp_f32 v31, v31\n\t"
        "v_exp_f32 v0, v0\n\t"
        "v_exp_f32 v1, v1\n\t"
        "v_smfmac_f32_32x32x32_bf16 v[80:95], v[124:127], v[24:31], v128\n\t"
        "s_mov_b64 exec, %[vm]\n\t"
        "ds_read_b128 v[120:123], v130 offset:3072\n\t"
        "ds_read_b128 v[124:127], v130 offset:3584\n\t"
        "s_mov_b64 exec, -1\n\t"
        "v_exp_f32 v2, v2\n\t"
        "v_exp_f32 v3, v3\n\t"
        "v_exp_f32 v4, v4\n\t"
        "v_exp_f32 v5, v5\n\t"
        "v_exp_f32 v6, v6\n\t"
        "v_mfma_f32_32x32x16_bf16 v[16:31], v[104:107], v[100:103], v[48:63]\n\t"
        "ds_read_b128 v[104:107], v129 offset:4096\n\t"
        "v_exp_f32 v7, v7\n\t"
        "v_exp_f32 v8, v8\n\t"
        "v_exp_f32 v9, v9\n\t"
        "v_exp_f32 v10, v10\n\t"
        "v_exp_f32 v11, v11\n\t"
        "v_smfmac_f32_32x32x32_bf16 v[64:79], v[112:115], v[0:7], v128\n\t"
        "v_exp_f32 v12, v12\n\t"
        "v_exp_f32 v13, v13\n\t"
        "v_exp_f32 v14, v14\n\t"
        "v_exp_f32 v15, v15\n\t"
        "v_exp_f32 v16, v16\n\t"
        "v_exp_f32 v17, v17\n\t"
        "v_smfmac_f32_32x32x32_bf16 v[64:79], v[116:119], v[8:15], v128\n\t"
        "v_exp_f32 v18, v18\n\t"
        "v_exp_f32 v19, v19\n\t"
        "v_exp_f32 v20, v20\n\t"
        "v_exp_f32 v21, v21\n\t"
        "v_mfma_f32_32x32x16_bf16 v[0:15], v[108:111], v[96:99], v[32:47]\n\t"
        "v_exp_f32 v22, v22\n\t"
        "v_exp_f32 v23, v23\n\t"
        "v_exp_f32 v24, v24\n\t"
        "v_exp_f32 v25, v25\n\t"
        "v_exp_f32 v26, v26\n\t"
        "v_exp_f32 v27, v27\n\t"
        "v_smfmac_f32_32x32x32_bf16 v[80:95], v[112:115], v[16:23], v128\n\t"
        "s_waitcnt lgkmcnt(0)\n\t"
        "v_exp_f32 v28, v28\n\t"
        "v_exp_f32 v29, v29\n\t"
        "v_exp_f32 v30, v30\n\t"
        "v_exp_f32 v31, v31\n\t"
        "v_exp_f32 v0, v0\n\t"
        "v_exp_f32 v1, v1\n\t"
        "v_smfmac_f32_32x32x32_bf16 v[80:95], v[116:119], v[24:31], v128\n\t"
        "s_mov_b64 exec, %[vm]\n\t"
        "ds_read_b128 v[112:115], v130 offset:4096\n\t"
        "ds_read_b128 v[116:119], v130 offset:4608\n\t"
        "s_mov_b64 exec, -1\n\t"
        "v_exp_f32 v2, v2\n\t"
        "v_exp_f32 v3, v3\n\t"
        "v_exp_f32 v4, v4\n\t"
        "v_exp_f32 v5, v5\n\t"
        "v_exp_f32 v6, v6\n\t"
        "v_mfma_f32_32x32x16_bf16 v[16:31], v[108:111], v[100:103], v[48:63]\n\t"
        "ds_read_b128 v[108:111], v129 offset:5120\n\t"
        "v_exp_f32 v7, v7\n\t"
        "v_exp_f32 v8, v8\n\t"
        "v_exp_f32 v9, v9\n\t"
        "v_exp_f32 v10, v10\n\t"
        "v_exp_f32 v11, v11\n\t"
        "v_smfmac_f32_32x32x32_bf16 v[64:79], v[120:123], v[0:7], v128\n\t"
        "v_exp_f32 v12, v12\n\t"
        "v_exp_f32 v13, v13\n\t"
        "v_exp_f32 v14, v14\n\t"
        "v_exp_f32 v15, v15\n\t"
        "v_exp_f32 v16, v16\n\t"
        "v_exp_f32 v17, v17\n\t"
        "v_smfmac_f32_32x32x32_bf16 v[64:79], v[124:127], v[8:15], v128\n\t"
        "v_exp_f32 v18, v18\n\t"
        "v_exp_f32 v19, v19\n\t"
        "v_exp_f32 v20, v20\n\t"
        "v_exp_f32 v21, v21\n\t"
        "v_mfma_f32_32x32x16_bf16 v[0:15], v[104:107], v[96:99], v[32:47]\n\t"
        "v_exp_f32 v22, v22\n\t"
        "v_exp_f32 v23, v23\n\t"
        "v_exp_f32 v24, v24\n\t"
        "v_exp_f32 v25, v25\n\t"
        "v_exp_f32 v26, v26\n\t"
        "v_exp_f32 v27, v27\n\t"
        "v_smfmac_f32_32x32x32_bf16 v[80:95], v[120:123], v[16:23], v128\n\t"
        "s_waitcnt lgkmcnt(0)\n\t"
          ATT_IO);
      if (it == 0) { if (loader) WAIT_VM(0); } else if (it == 1) WAIT_VM(8); else if (it == 2) WAIT_VM(16);
      asm volatile("s_barrier" ::: "memory");
      if (it == 0) {
        if (loader) {
          if (lane == 0) put_flag(wsr, flag_off(F_P, tb, hg) + hd * 8, tag);
          issue_kv(2); issue_kv(3);
        } else {
#pragma unroll
          for (int t = 0; t < 4; ++t) *(LAS u32x2*)(smb + L_PQ + imgw + t * 32) = pk4(phi4(tile4(2 * t + 1)));
        }
      } else if (it == 1) {
        if (loader) {
          unsigned so = (256 + (w - 4) * 64) * 1024;
          asm volatile(
        "buffer_load_dwordx4 v[132:135], %[vo], %[r], %[so] offen\n\t"
        "s_add_u32 %[so], %[so], 0x2000\n\t"
        "buffer_load_dwordx4 v[136:139], %[vo], %[r], %[so] offen\n\t"
        "s_add_u32 %[so], %[so], 0x2000\n\t"
        "buffer_load_dwordx4 v[140:143], %[vo], %[r], %[so] offen\n\t"
        "s_add_u32 %[so], %[so], 0x2000\n\t"
        "buffer_load_dwordx4 v[144:147], %[vo], %[r], %[so] offen\n\t"
        "s_add_u32 %[so], %[so], 0x2000\n\t"
        "buffer_load_dwordx4 v[148:151], %[vo], %[r], %[so] offen\n\t"
        "s_add_u32 %[so], %[so], 0x2000\n\t"
        "buffer_load_dwordx4 v[152:155], %[vo], %[r], %[so] offen\n\t"
        "s_add_u32 %[so], %[so], 0x2000\n\t"
        "buffer_load_dwordx4 v[156:159], %[vo], %[r], %[so] offen\n\t"
        "s_add_u32 %[so], %[so], 0x2000\n\t"
        "buffer_load_dwordx4 v[160:163], %[vo], %[r], %[so] offen\n\t"
              : [so] "+s"(so) : [vo] "v"(ovo), [r] "s"(mkr(wob + hg * 128, 511 * 1024 + 128)) : "memory", "scc");
          issue_wo(0);
        } else if (w == 0) {
          float s;
          asm volatile(
        "s_waitcnt vmcnt(0)\n\t"
        "v_add_f32 %[s], v132, v133\n\t"
        "v_add_f32 %[s], %[s], v134\n\t"
        "v_add_f32 %[s], %[s], v135\n\t"
        "v_add_f32 %[s], %[s], v136\n\t"
        "v_add_f32 %[s], %[s], v137\n\t"
        "v_add_f32 %[s], %[s], v138\n\t"
        "v_add_f32 %[s], %[s], v139\n\t"
        "v_add_f32 %[s], %[s], v140\n\t"
        "v_add_f32 %[s], %[s], v141\n\t"
        "v_add_f32 %[s], %[s], v142\n\t"
        "v_add_f32 %[s], %[s], v143\n\t"
        "v_add_f32 %[s], %[s], v144\n\t"
        "v_add_f32 %[s], %[s], v145\n\t"
        "v_add_f32 %[s], %[s], v146\n\t"
        "v_add_f32 %[s], %[s], v147\n\t"
        "v_add_f32 %[s], %[s], v148\n\t"
        "v_add_f32 %[s], %[s], v149\n\t"
        "v_add_f32 %[s], %[s], v150\n\t"
        "v_add_f32 %[s], %[s], v151\n\t"
        "v_add_f32 %[s], %[s], v152\n\t"
        "v_add_f32 %[s], %[s], v153\n\t"
        "v_add_f32 %[s], %[s], v154\n\t"
        "v_add_f32 %[s], %[s], v155\n\t"
        "v_add_f32 %[s], %[s], v156\n\t"
        "v_add_f32 %[s], %[s], v157\n\t"
        "v_add_f32 %[s], %[s], v158\n\t"
        "v_add_f32 %[s], %[s], v159\n\t"
        "v_add_f32 %[s], %[s], v160\n\t"
        "v_add_f32 %[s], %[s], v161\n\t"
        "v_add_f32 %[s], %[s], v162\n\t"
        "v_add_f32 %[s], %[s], v163\n\t"
              : [s] "=&v"(s) :: "memory");
          if (lane < 34) __builtin_amdgcn_raw_buffer_store_b32(__float_as_uint(s), wsr, WS_KVS + hg * 4352 + (34 * tb + lane) * 4, 0, 16);
        }
      } else if (it == 2) {
        if (loader) issue_wo(1);
      } else {
        if (loader) issue_wo(2);
        else { WAIT_VM(0); if (lane == 0) put_flag(wsr, flag_off(F_OA, tb, hg) + hd * 8, tag); }
      }
      asm volatile(
        "v_exp_f32 v28, v28\n\t"
        "v_exp_f32 v29, v29\n\t"
        "v_exp_f32 v30, v30\n\t"
        "v_exp_f32 v31, v31\n\t"
        "v_exp_f32 v0, v0\n\t"
        "v_exp_f32 v1, v1\n\t"
        "v_smfmac_f32_32x32x32_bf16 v[80:95], v[124:127], v[24:31], v128\n\t"
        "s_mov_b64 exec, %[vm]\n\t"
        "ds_read_b128 v[120:123], v130 offset:5120\n\t"
        "ds_read_b128 v[124:127], v130 offset:5632\n\t"
        "s_mov_b64 exec, -1\n\t"
        "v_exp_f32 v2, v2\n\t"
        "v_exp_f32 v3, v3\n\t"
        "v_exp_f32 v4, v4\n\t"
        "v_exp_f32 v5, v5\n\t"
        "v_exp_f32 v6, v6\n\t"
        "v_mfma_f32_32x32x16_bf16 v[16:31], v[104:107], v[100:103], v[48:63]\n\t"
        "ds_read_b128 v[104:107], v129 offset:6144\n\t"
        "v_exp_f32 v7, v7\n\t"
        "v_exp_f32 v8, v8\n\t"
        "v_exp_f32 v9, v9\n\t"
        "v_exp_f32 v10, v10\n\t"
        "v_exp_f32 v11, v11\n\t"
        "v_smfmac_f32_32x32x32_bf16 v[64:79], v[112:115], v[0:7], v128\n\t"
        "v_exp_f32 v12, v12\n\t"
        "v_exp_f32 v13, v13\n\t"
        "v_exp_f32 v14, v14\n\t"
        "v_exp_f32 v15, v15\n\t"
        "v_exp_f32 v16, v16\n\t"
        "v_exp_f32 v17, v17\n\t"
        "v_smfmac_f32_32x32x32_bf16 v[64:79], v[116:119], v[8:15], v128\n\t"
        "v_exp_f32 v18, v18\n\t"
        "v_exp_f32 v19, v19\n\t"
        "v_exp_f32 v20, v20\n\t"
        "v_exp_f32 v21, v21\n\t"
        "v_mfma_f32_32x32x16_bf16 v[0:15], v[108:111], v[96:99], v[32:47]\n\t"
        "v_exp_f32 v22, v22\n\t"
        "v_exp_f32 v23, v23\n\t"
        "v_exp_f32 v24, v24\n\t"
        "v_exp_f32 v25, v25\n\t"
        "v_exp_f32 v26, v26\n\t"
        "v_exp_f32 v27, v27\n\t"
        "v_smfmac_f32_32x32x32_bf16 v[80:95], v[112:115], v[16:23], v128\n\t"
        "s_waitcnt lgkmcnt(0)\n\t"
        "v_exp_f32 v28, v28\n\t"
        "v_exp_f32 v29, v29\n\t"
        "v_exp_f32 v30, v30\n\t"
        "v_exp_f32 v31, v31\n\t"
        "v_exp_f32 v0, v0\n\t"
        "v_exp_f32 v1, v1\n\t"
        "v_smfmac_f32_32x32x32_bf16 v[80:95], v[116:119], v[24:31], v128\n\t"
        "s_mov_b64 exec, %[vm]\n\t"
        "ds_read_b128 v[112:115], v130 offset:6144\n\t"
        "ds_read_b128 v[116:119], v130 offset:6656\n\t"
        "s_mov_b64 exec, -1\n\t"
        "v_exp_f32 v2, v2\n\t"
        "v_exp_f32 v3, v3\n\t"
        "v_exp_f32 v4, v4\n\t"
        "v_exp_f32 v5, v5\n\t"
        "v_exp_f32 v6, v6\n\t"
        "v_mfma_f32_32x32x16_bf16 v[16:31], v[108:111], v[100:103], v[48:63]\n\t"
        "ds_read_b128 v[108:111], v129 offset:7168\n\t"
        "v_exp_f32 v7, v7\n\t"
        "v_exp_f32 v8, v8\n\t"
        "v_exp_f32 v9, v9\n\t"
        "v_exp_f32 v10, v10\n\t"
        "v_exp_f32 v11, v11\n\t"
        "v_smfmac_f32_32x32x32_bf16 v[64:79], v[120:123], v[0:7], v128\n\t"
        "v_exp_f32 v12, v12\n\t"
        "v_exp_f32 v13, v13\n\t"
        "v_exp_f32 v14, v14\n\t"
        "v_exp_f32 v15, v15\n\t"
        "v_exp_f32 v16, v16\n\t"
        "v_exp_f32 v17, v17\n\t"
        "v_smfmac_f32_32x32x32_bf16 v[64:79], v[124:127], v[8:15], v128\n\t"
        "v_exp_f32 v18, v18\n\t"
        "v_exp_f32 v19, v19\n\t"
        "v_exp_f32 v20, v20\n\t"
        "v_exp_f32 v21, v21\n\t"
        "v_mfma_f32_32x32x16_bf16 v[0:15], v[104:107], v[96:99], v[32:47]\n\t"
        "v_exp_f32 v22, v22\n\t"
        "v_exp_f32 v23, v23\n\t"
        "v_exp_f32 v24, v24\n\t"
        "v_exp_f32 v25, v25\n\t"
        "v_exp_f32 v26, v26\n\t"
        "v_exp_f32 v27, v27\n\t"
        "v_smfmac_f32_32x32x32_bf16 v[80:95], v[120:123], v[16:23], v128\n\t"
        "s_waitcnt lgkmcnt(0)\n\t"
        "v_exp_f32 v28, v28\n\t"
        "v_exp_f32 v29, v29\n\t"
        "v_exp_f32 v30, v30\n\t"
        "v_exp_f32 v31, v31\n\t"
        "v_exp_f32 v0, v0\n\t"
        "v_exp_f32 v1, v1\n\t"
        "v_smfmac_f32_32x32x32_bf16 v[80:95], v[124:127], v[24:31], v128\n\t"
        "s_mov_b64 exec, %[vm]\n\t"
        "ds_read_b128 v[120:123], v130 offset:7168\n\t"
        "ds_read_b128 v[124:127], v130 offset:7680\n\t"
        "s_mov_b64 exec, -1\n\t"
        "v_exp_f32 v2, v2\n\t"
        "v_exp_f32 v3, v3\n\t"
        "v_exp_f32 v4, v4\n\t"
        "v_exp_f32 v5, v5\n\t"
        "v_exp_f32 v6, v6\n\t"
        "v_mfma_f32_32x32x16_bf16 v[16:31], v[104:107], v[100:103], v[48:63]\n\t"
        "ds_read_b128 v[104:107], v129 offset:32768\n\t"
        "v_exp_f32 v7, v7\n\t"
        "v_exp_f32 v8, v8\n\t"
        "v_exp_f32 v9, v9\n\t"
        "v_exp_f32 v10, v10\n\t"
        "v_exp_f32 v11, v11\n\t"
        "v_smfmac_f32_32x32x32_bf16 v[64:79], v[112:115], v[0:7], v128\n\t"
        "v_exp_f32 v12, v12\n\t"
        "v_exp_f32 v13, v13\n\t"
        "v_exp_f32 v14, v14\n\t"
        "v_exp_f32 v15, v15\n\t"
        "v_exp_f32 v16, v16\n\t"
        "v_exp_f32 v17, v17\n\t"
        "v_smfmac_f32_32x32x32_bf16 v[64:79], v[116:119], v[8:15], v128\n\t"
        "v_exp_f32 v18, v18\n\t"
        "v_exp_f32 v19, v19\n\t"
        "v_exp_f32 v20, v20\n\t"
        "v_exp_f32 v21, v21\n\t"
        "v_mfma_f32_32x32x16_bf16 v[0:15], v[108:111], v[96:99], v[32:47]\n\t"
        "v_exp_f32 v22, v22\n\t"
        "v_exp_f32 v23, v23\n\t"
        "v_exp_f32 v24, v24\n\t"
        "v_exp_f32 v25, v25\n\t"
        "v_exp_f32 v26, v26\n\t"
        "v_exp_f32 v27, v27\n\t"
        "v_smfmac_f32_32x32x32_bf16 v[80:95], v[112:115], v[16:23], v128\n\t"
        "s_waitcnt lgkmcnt(0)\n\t"
        "v_exp_f32 v28, v28\n\t"
        "v_exp_f32 v29, v29\n\t"
        "v_exp_f32 v30, v30\n\t"
        "v_exp_f32 v31, v31\n\t"
        "v_exp_f32 v0, v0\n\t"
        "v_exp_f32 v1, v1\n\t"
        "v_smfmac_f32_32x32x32_bf16 v[80:95], v[116:119], v[24:31], v128\n\t"
        "s_mov_b64 exec, %[vm]\n\t"
        "ds_read_b128 v[112:115], v130 offset:32768\n\t"
        "ds_read_b128 v[116:119], v130 offset:33280\n\t"
        "s_mov_b64 exec, -1\n\t"
        "v_exp_f32 v2, v2\n\t"
        "v_exp_f32 v3, v3\n\t"
        "v_exp_f32 v4, v4\n\t"
        "v_exp_f32 v5, v5\n\t"
        "v_exp_f32 v6, v6\n\t"
        "v_mfma_f32_32x32x16_bf16 v[16:31], v[108:111], v[100:103], v[48:63]\n\t"
        "ds_read_b128 v[108:111], v129 offset:33792\n\t"
        "v_exp_f32 v7, v7\n\t"
        "v_exp_f32 v8, v8\n\t"
        "v_exp_f32 v9, v9\n\t"
        "v_exp_f32 v10, v10\n\t"
        "v_exp_f32 v11, v11\n\t"
        "v_smfmac_f32_32x32x32_bf16 v[64:79], v[120:123], v[0:7], v128\n\t"
        "v_exp_f32 v12, v12\n\t"
        "v_exp_f32 v13, v13\n\t"
        "v_exp_f32 v14, v14\n\t"
        "v_exp_f32 v15, v15\n\t"
        "v_exp_f32 v16, v16\n\t"
        "v_exp_f32 v17, v17\n\t"
        "v_smfmac_f32_32x32x32_bf16 v[64:79], v[124:127], v[8:15], v128\n\t"
        "v_exp_f32 v18, v18\n\t"
        "v_exp_f32 v19, v19\n\t"
        "v_exp_f32 v20, v20\n\t"
        "v_exp_f32 v21, v21\n\t"
        "v_mfma_f32_32x32x16_bf16 v[0:15], v[104:107], v[96:99], v[32:47]\n\t"
        "v_exp_f32 v22, v22\n\t"
        "v_exp_f32 v23, v23\n\t"
        "v_exp_f32 v24, v24\n\t"
        "v_exp_f32 v25, v25\n\t"
        "v_exp_f32 v26, v26\n\t"
        "v_exp_f32 v27, v27\n\t"
        "v_smfmac_f32_32x32x32_bf16 v[80:95], v[120:123], v[16:23], v128\n\t"
        "s_waitcnt lgkmcnt(0)\n\t"
        "v_add_u32 v129, 0x8000, v129\n\t"
        "v_add_u32 v130, 0x8000, v130\n\t"
          ATT_IO);
      if (it == 0) {
        if (w == 0) {
          poll2(wsr, flag_off(F_P, l31, hg) + h * 16, tag);
          unsigned so = WS_KVP + hg * 4352 + 34 * tb * 4;
          asm volatile(
        "buffer_load_dword v132, %[vo], %[r], %[so] offen sc1\n\t"
        "s_add_u32 %[so], %[so], 34816\n\t"
        "buffer_load_dword v133, %[vo], %[r], %[so] offen sc1\n\t"
        "s_add_u32 %[so], %[so], 34816\n\t"
        "buffer_load_dword v134, %[vo], %[r], %[so] offen sc1\n\t"
        "s_add_u32 %[so], %[so], 34816\n\t"
        "buffer_load_dword v135, %[vo], %[r], %[so] offen sc1\n\t"
        "s_add_u32 %[so], %[so], 34816\n\t"
        "buffer_load_dword v136, %[vo], %[r], %[so] offen sc1\n\t"
        "s_add_u32 %[so], %[so], 34816\n\t"
        "buffer_load_dword v137, %[vo], %[r], %[so] offen sc1\n\t"
        "s_add_u32 %[so], %[so], 34816\n\t"
        "buffer_load_dword v138, %[vo], %[r], %[so] offen sc1\n\t"
        "s_add_u32 %[so], %[so], 34816\n\t"
        "buffer_load_dword v139, %[vo], %[r], %[so] offen sc1\n\t"
        "s_add_u32 %[so], %[so], 34816\n\t"
        "buffer_load_dword v140, %[vo], %[r], %[so] offen sc1\n\t"
        "s_add_u32 %[so], %[so], 34816\n\t"
        "buffer_load_dword v141, %[vo], %[r], %[so] offen sc1\n\t"
        "s_add_u32 %[so], %[so], 34816\n\t"
        "buffer_load_dword v142, %[vo], %[r], %[so] offen sc1\n\t"
        "s_add_u32 %[so], %[so], 34816\n\t"
        "buffer_load_dword v143, %[vo], %[r], %[so] offen sc1\n\t"
        "s_add_u32 %[so], %[so], 34816\n\t"
        "buffer_load_dword v144, %[vo], %[r], %[so] offen sc1\n\t"
        "s_add_u32 %[so], %[so], 34816\n\t"
        "buffer_load_dword v145, %[vo], %[r], %[so] offen sc1\n\t"
        "s_add_u32 %[so], %[so], 34816\n\t"
        "buffer_load_dword v146, %[vo], %[r], %[so] offen sc1\n\t"
        "s_add_u32 %[so], %[so], 34816\n\t"
        "buffer_load_dword v147, %[vo], %[r], %[so] offen sc1\n\t"
        "s_add_u32 %[so], %[so], 34816\n\t"
        "buffer_load_dword v148, %[vo], %[r], %[so] offen sc1\n\t"
        "s_add_u32 %[so], %[so], 34816\n\t"
        "buffer_load_dword v149, %[vo], %[r], %[so] offen sc1\n\t"
        "s_add_u32 %[so], %[so], 34816\n\t"
        "buffer_load_dword v150, %[vo], %[r], %[so] offen sc1\n\t"
        "s_add_u32 %[so], %[so], 34816\n\t"
        "buffer_load_dword v151, %[vo], %[r], %[so] offen sc1\n\t"
        "s_add_u32 %[so], %[so], 34816\n\t"
        "buffer_load_dword v152, %[vo], %[r], %[so] offen sc1\n\t"
        "s_add_u32 %[so], %[so], 34816\n\t"
        "buffer_load_dword v153, %[vo], %[r], %[so] offen sc1\n\t"
        "s_add_u32 %[so], %[so], 34816\n\t"
        "buffer_load_dword v154, %[vo], %[r], %[so] offen sc1\n\t"
        "s_add_u32 %[so], %[so], 34816\n\t"
        "buffer_load_dword v155, %[vo], %[r], %[so] offen sc1\n\t"
        "s_add_u32 %[so], %[so], 34816\n\t"
        "buffer_load_dword v156, %[vo], %[r], %[so] offen sc1\n\t"
        "s_add_u32 %[so], %[so], 34816\n\t"
        "buffer_load_dword v157, %[vo], %[r], %[so] offen sc1\n\t"
        "s_add_u32 %[so], %[so], 34816\n\t"
        "buffer_load_dword v158, %[vo], %[r], %[so] offen sc1\n\t"
        "s_add_u32 %[so], %[so], 34816\n\t"
        "buffer_load_dword v159, %[vo], %[r], %[so] offen sc1\n\t"
        "s_add_u32 %[so], %[so], 34816\n\t"
        "buffer_load_dword v160, %[vo], %[r], %[so] offen sc1\n\t"
        "s_add_u32 %[so], %[so], 34816\n\t"
        "buffer_load_dword v161, %[vo], %[r], %[so] offen sc1\n\t"
        "s_add_u32 %[so], %[so], 34816\n\t"
        "buffer_load_dword v162, %[vo], %[r], %[so] offen sc1\n\t"
        "s_add_u32 %[so], %[so], 34816\n\t"
        "buffer_load_dword v163, %[vo], %[r], %[so] offen sc1\n\t"
              : [so] "+s"(so) : [vo] "v"((lane < 34 ? lane : 33) * 4), [r] "s"(wsr) : "memory", "scc");
        } else if (w == 1) {
          *(LAS unsigned*)(smb + L_GT + lane * 4) = __builtin_amdgcn_raw_buffer_load_b32(wsr, WS_G + (tb * 64 + lane) * 8, 0, 16);
        }
      } else if (it == 1) {
        if (w == 0) { WAIT_VM(0); if (lane == 0) put_flag(wsr, flag_off(F_S, tb, hg), tag); }
      } else if (it == 2) {
        if (!loader) {
          poll(wsr, flag_off(F_S, l31, hg), tag);
          const unsigned vo = WS_KVS + hg * 4352 + (hd * 272 + (l31 < 16 ? l31 : 16) * 16 + h * 8) * 4;
          const f32x4 s0 = __builtin_bit_cast(f32x4, __builtin_amdgcn_raw_buffer_load_b128(wsr, vo, 0, 16));
          const f32x4 s1 = __builtin_bit_cast(f32x4, __builtin_amdgcn_raw_buffer_load_b128(wsr, vo, 16, 16));
          const u32x4 pq0 = ldstr2(smb + L_PQ + imgr, smb + L_PQ + imgr + 4 * RP), pq1 = ldstr2(smb + L_PQ + imgr + 64, smb + L_PQ + imgr + 64 + 4 * RP);
          const float g0 = *(const LAS float*)(smb + L_GT + l31 * 4), go = *(const LAS float*)(smb + L_GT + 128 + l31 * 4);
          const u32x4 a = {pk2(s0[0], s0[1]), pk2(s0[2], s0[3]), pk2(s1[0], s1[1]), pk2(s1[2], s1[3])};
          const f32x16 n0 = mf32(a, pq0, zero16), n1 = mf32(a, pq1, zero16);
          const unsigned oo = WS_OA + (unsigned)(tb * 64 + hg * 4 + hd) * 1024 + lane * 16;
          __builtin_amdgcn_raw_buffer_store_b128(pk8(n0, 0, g0 * __builtin_amdgcn_rcpf(n0[8] + 1e-6f)), wsr, oo, 0, 16);
          __builtin_amdgcn_raw_buffer_store_b128(pk8(n1, 0, go * __builtin_amdgcn_rcpf(n1[8] + 1e-6f)), wsr, oo, 32768, 16);
        }
      }
    }
    const unsigned xw = smb + L_E + w * 2304, xr = smb + L_E + (w ^ 4) * 2304;
    auto send = [&]() {
      *(LAS f32x4*)(xw + lane * 16) = (f32x4){aa[0], aa[1], aa[2], aa[3]};
      *(LAS f32x4*)(xw + 1024 + lane * 16) = (f32x4){aa[4], aa[5], aa[6], aa[7]};
      *(LAS float*)(xw + 2048 + lane * 4) = aa[8];
    };
    send();
    asm volatile(
        "v_exp_f32 v28, v28\n\t"
        "v_exp_f32 v29, v29\n\t"
        "v_exp_f32 v30, v30\n\t"
        "v_exp_f32 v31, v31\n\t"
        "s_nop 1\n\t"
        "v_smfmac_f32_32x32x32_bf16 v[80:95], v[124:127], v[24:31], v128\n\t"
        "s_nop 15\n\t"
        "s_nop 3\n\t"
        ATT_IO);
    {
      const bool anyb = __builtin_amdgcn_ballot_w64(!(aa[8] < 1e18f) || !(ab[8] < 1e18f)) != 0;
      *(LAS unsigned char*)(smb + L_E + 18432 + w) = anyb;
    }
    if (!loader) {
      poll(wsr, flag_off(F_OA, tb, 2 * (w & 3) + ((lane >> 2) & 1)) + (lane & 3) * 8, tag);
      load_ofa();
    } else {
#pragma unroll
      for (int s = 0; s < 8; ++s) asm volatile("" : "=v"(ofa[s]));
      WAIT_VM(8);
    }
    BAR();
    const u32x2 bw = *(const LAS u32x2*)(smb + L_E + 18432);
    f32x4 r0 = *(const LAS f32x4*)(xr + lane * 16), r1 = *(const LAS f32x4*)(xr + 1024 + lane * 16);
    float r2 = *(const LAS float*)(xr + 2048 + lane * 4);
    gt = *(const LAS float*)(smb + L_GT + kh * 128 + l31 * 4);
    __builtin_amdgcn_sched_barrier(0);
    if (__builtin_expect(__builtin_amdgcn_readfirstlane(bw[0] | bw[1]) != 0, 0)) {
      WAIT_VM(0);
      BAR();
      if (loader) { issue_kv(0); issue_kv(1); issue_kv(2); issue_kv(3); WAIT_VM(0); }
      BAR();
#pragma unroll 1
      for (int t = 0; t < 64; ++t) {
        const u32x4 kf = lds128(smb + (t >> 4) * 32768 + (t & 15) * 1024 + lane * 16);
        f32x16 ta = mf32(kf, qa, zero16), tc = mf32(kf, qb, zero16);
#pragma unroll
        for (int i = 0; i < 16; ++i) { ma = fmaxf(ma, ta[i]); mb = fmaxf(mb, tc[i]); }
      }
      ma = xhalf_max(ma); mb = xhalf_max(mb);
      aa = zero16; ab = zero16;
#pragma unroll 1
      for (int t = 0; t < 32; ++t) {
        const unsigned tbase = smb + (t >> 3) * 32768 + kh * 8192 + (t & 7) * 1024;
        const u32x4 kf = lds128(tbase + lane * 16);
        u32x4 v0 = lds128(tbase + 16384 + h * 256 + l15 * 16), v1 = lds128(tbase + 16384 + 512 + h * 256 + l15 * 16);
        if (lane & 16) { v0 = (u32x4){one, one, one, one}; v1 = v0; }
        f32x16 ta = mf32(kf, qa, zero16), tc = mf32(kf, qb, zero16);
        f32x8 p0, p1, p2, p3;
#pragma unroll
        for (int i = 0; i < 8; ++i) {
          p0[i] = __builtin_amdgcn_exp2f(ta[i] - ma); p1[i] = __builtin_amdgcn_exp2f(ta[8 + i] - ma);
          p2[i] = __builtin_amdgcn_exp2f(tc[i] - mb); p3[i] = __builtin_amdgcn_exp2f(tc[8 + i] - mb);
        }
        aa = smf32(v1, p1, smf32(v0, p0, aa)); ab = smf32(v1, p3, smf32(v0, p2, ab));
      }
      BAR();
      if (loader) { issue_wo(0); issue_wo(1); issue_wo(2); }
      send();
      WAIT_VM(0);
      BAR();
      r0 = *(const LAS f32x4*)(xr + lane * 16); r1 = *(const LAS f32x4*)(xr + 1024 + lane * 16);
      r2 = *(const LAS float*)(xr + 2048 + lane * 4);
    }
#pragma unroll
    for (int i = 0; i < 4; ++i) { o[i] = ab[i] + r0[i]; o[4 + i] = ab[4 + i] + r1[i]; }
    o[8] = ab[8] + r2;
    wo3a = bf0; wo3b = bf1;
  }
  {
    const float sc = (1.f - gt) * __builtin_amdgcn_rcpf(o[8]);
    u32x4 v = {pk2(o[0] * sc, o[1] * sc), pk2(o[2] * sc, o[3] * sc), pk2(o[4] * sc, o[5] * sc), pk2(o[6] * sc, o[7] * sc)};
#pragma unroll
    for (int i = 0; i < 4; ++i) v[i] = v[i] < 0xfffffffeu ? v[i] : 0xfffffffeu;
    __builtin_amdgcn_raw_buffer_store_b128(v, wsr, obo, 0, 16);
    *(LAS u32x4*)(smb + L_PQ + w * 1024 + lane * 16) = v;
  }
  if (loader) load_ofa();
  const int tt = w >> 2, kq = w & 3;
  const unsigned fo = (unsigned)((tb * 2 + tt) * 32 + 8 * kq) * 1024 + lane * 16;
  u32x4 of[8];
#pragma unroll
  for (int s = 0; s < 8; ++s) of[s] = ofa[s];
  if (loader) {
#pragma unroll
    for (int j = 0; j < 8; ++j) {
      const u32x16& q = j < 4 ? wo3a : wo3b;
      *(LAS u32x4*)(smb + 98304 + ((w - 4) * 8 + j) * 1024 + lane * 16) = (u32x4){q[4 * (j & 3)], q[4 * (j & 3) + 1], q[4 * (j & 3) + 2], q[4 * (j & 3) + 3]};
    }
  }
  f32x16 c0 = zero16, c1 = zero16;
  const unsigned wob0 = smb + kq * 16384 + xo * 2048 + h * 512 + (l15 >> 2) * 64 + (g & 1) * 32 + (lane & 3) * 8;
#pragma unroll
  for (int s = 0; s < 8; ++s) {
    const unsigned a = wob0 ^ (s * 2048);
    c0 = mf32(ldstr2(a, a + 1024), of[s], c0);
    c1 = mf32(ldstr2(a + 256, a + 1024 + 256), of[s], c1);
  }
#define LOAD_OB _Pragma("unroll") for (int s = 0; s < 8; ++s) of[s] = __builtin_amdgcn_raw_buffer_load_b128(wsr, fo, (2 * kq + ((s ^ xo) >> 2) == hg ? WS_OA : WS_OB) + (s ^ xo) * 1024, 16); \
  __builtin_amdgcn_sched_barrier(0);
  BAR();
  u32x4 wf[8][2];
  unsigned wob1 = wob0 + 65536;
  asm volatile("" : "+v"(wob1));
#pragma unroll
  for (int s = 0; s < 8; ++s) {
    const unsigned a = wob1 ^ (s * 2048);
    wf[s][0] = ldstr2(a, a + 1024);
    wf[s][1] = ldstr2(a + 256, a + 1024 + 256);
  }
LOAD_OB
#pragma unroll
  for (int q = 0; q < 2; ++q) {
    if (2 * kq + (q ^ (xo >> 2)) == hg) {
#pragma unroll
      for (int s = 0; s < 4; ++s) of[4 * q + s] = lds128(smb + L_PQ + tt * 4096 + (s ^ (xo & 3)) * 1024 + lane * 16);
    }
    const f32x16 a0 = c0, a1 = c1;
    auto step4 = [&]() {
      unsigned mx = 0;
      c0 = a0; c1 = a1;
#pragma unroll
      for (int s = 4 * q; s < 4 * q + 4; ++s) {
        c0 = mf32(wf[s][0], of[s], c0); c1 = mf32(wf[s][1], of[s], c1);
        mx = max(max(mx, of[s][0]), of[s][1]); mx = max(max(mx, of[s][2]), of[s][3]);
        __builtin_amdgcn_sched_barrier(0);
      }
      return __builtin_amdgcn_ballot_w64(mx == ~0u) != 0;
    };
    if (step4()) {
#pragma unroll 1
      for (unsigned spins = 0; spins < (1u << 20); ++spins) {
        __builtin_amdgcn_s_sleep(RETRY_SLEEP);
#pragma unroll
        for (int s = 4 * q; s < 4 * q + 4; ++s) of[s] = __builtin_amdgcn_raw_buffer_load_b128(wsr, fo, WS_OB + (s ^ xo) * 1024, 16);
        __builtin_amdgcn_sched_barrier(0);
        if (!step4()) break;
      }
    }
  }
  {
    const unsigned rb = smb + tt * 32768 + lane * 16;
#pragma unroll
    for (int d = 0; d < 4; ++d) {
      const f32x16& c = d < 2 ? c0 : c1;
      const int r0 = 8 * (d & 1);
      *(LAS f32x4*)(rb + kq * 2048 + d * 8192) = (f32x4){c[r0], c[r0 + 1], c[r0 + 2], c[r0 + 3]};
      *(LAS f32x4*)(rb + kq * 2048 + d * 8192 + 1024) = (f32x4){c[r0 + 4], c[r0 + 5], c[r0 + 6], c[r0 + 7]};
    }
    BAR();
    f32x4 pa[4], pb[4];
#pragma unroll
    for (int j = 0; j < 4; ++j) { pa[j] = *(const LAS f32x4*)(rb + kq * 8192 + j * 2048); pb[j] = *(const LAS f32x4*)(rb + kq * 8192 + j * 2048 + 1024); }
    const f32x4 s0 = (pa[0] + pa[1]) + (pa[2] + pa[3]), s1 = (pb[0] + pb[1]) + (pb[2] + pb[3]);
    unsigned e0 = pk2(s0[0], s0[1]), e1 = pk2(s0[2], s0[3]), b0 = pk2(s1[0], s1[1]), b1 = pk2(s1[2], s1[3]);
    auto r0 = __builtin_amdgcn_permlane32_swap(e0, b0, false, false);
    auto r1 = __builtin_amdgcn_permlane32_swap(e1, b1, false, false);
    *(u32x4*)(out + (size_t)(tb * 64 + tt * 32 + l31) * 1024 + hg * 128 + kq * 32 + h * 16) = (u32x4){r0[0], r1[0], r0[1], r1[1]};
  }
  if (blockIdx.x == 0 && tid == 0) put_flag(wsr, WS_EPOCH, tag);
  asm volatile("fk_code_1:");
}
}
extern "C" void kernel_launch(void* const* d_in, const int* in_sizes, int n_in, void* d_out, int out_size, void* d_ws, size_t ws_size,
                              hipStream_t stream) {
  fused_kernel<<<256, 512, 0, stream>>>((const char*)d_in[0], (const char*)d_in[1], (const char*)d_in[2], (const char*)d_in[4],
                                        (const char*)d_in[5], (const char*)d_in[6], (const char*)d_in[7], (const char*)d_in[8],
                                        (char*)d_ws, (char*)d_out);
}
```
